```python
import torch

def _softmax(x, axis=-1):
    return torch.softmax(x, dim=axis)
def _gelu(x):
    return torch.nn.functional.gelu(x, approximate="tanh")
def _silu(x):
    return torch.nn.functional.silu(x)
def _sigmoid(x):
    return torch.sigmoid(x)
def _layernorm(x, axis=-1, eps=1e-5):
    m = x.mean(dim=axis, keepdim=True)
    v = x.var(dim=axis, keepdim=True, unbiased=False)
    return (x - m) / torch.sqrt(v + eps)
def _scatter_add(tgt, idx, src, axis=0):
    return tgt.clone().index_add_(axis, idx, src)
def _segment_sum(src, seg, n):
    out = torch.zeros((n,) + src.shape[1:], dtype=src.dtype)
    return out.index_add_(0, seg, src)
def _pad(x, pw):
    flat = tuple(v for pair in reversed(pw) for v in pair)
    return torch.nn.functional.pad(x, flat)

kernel_name = 'standalone__convnext_block__vision'

def setup_inputs():

    _w = (lambda a: (lambda t: t.to(torch.bfloat16) if t.is_floating_point() else t)(torch.as_tensor(a)))
    H = W = 224
    C = 96
    return {
        "x": _w((lambda *s: torch.randn(*s, dtype=torch.bfloat16) * 0.05)(1, H, W, C)),
        "dw": _w((lambda *s: torch.randn(*s, dtype=torch.bfloat16) * 0.05)(7, 7, C)),
        "w1": _w((lambda *s: torch.randn(*s, dtype=torch.bfloat16) * 0.05)(C, 4 * C)),
        "w2": _w((lambda *s: torch.randn(*s, dtype=torch.bfloat16) * 0.05)(4 * C, C)),
    }

def reference(x, dw, w1, w2):
    B, H, W, C = x.shape
    pad = 3
    xp = _pad(x, ((0,0),(pad,pad),(pad,pad),(0,0)))
    out = torch.zeros_like(x)
    for i in range(7):
        for j in range(7):
            out = out + xp[:, i:i+H, j:j+W, :] * dw[i, j][None, None, None, :]
    h = _layernorm(out, axis=-1)
    return x + _gelu(h @ w1) @ w2
```

```cpp
#include <hip/hip_runtime.h>
#include <hip/hip_bf16.h>
#include <stdint.h>

constexpr int H = 224, W = 224, C = 96, HD = 384;
__device__ __forceinline__ float bf2f(uint16_t v) { return __uint_as_float(((uint32_t)v) << 16); }

__global__ __launch_bounds__(384) void naive_kernel(const uint16_t* __restrict__ x, const uint16_t* __restrict__ dw,
    const uint16_t* __restrict__ w1, const uint16_t* __restrict__ w2, __hip_bfloat16* __restrict__ out) {
  __shared__ float conv[4][C];
  __shared__ float hn[4][C];
  __shared__ float g[4][HD];
  int p0 = blockIdx.x * 4;
  int t = threadIdx.x;
  {
    int pp = t / C, c = t % C;
    int p = p0 + pp;
    int h = p / W, w = p % W;
    float acc = 0.f;
    for (int i = 0; i < 7; ++i) {
      int hh = h + i - 3;
      if (hh < 0 || hh >= H) continue;
      for (int j = 0; j < 7; ++j) {
        int ww = w + j - 3;
        if (ww < 0 || ww >= W) continue;
        acc += bf2f(x[(hh * W + ww) * C + c]) * bf2f(dw[(i * 7 + j) * C + c]);
      }
    }
    conv[pp][c] = acc;
  }
  __syncthreads();
  {
    int pp = t / C, c = t % C;
    float m = 0.f;
    for (int k = 0; k < C; ++k) m += conv[pp][k];
    m /= C;
    float v = 0.f;
    for (int k = 0; k < C; ++k) { float d = conv[pp][k] - m; v += d * d; }
    v /= C;
    float hv = (conv[pp][c] - m) * rsqrtf(v + 1e-5f);
    hn[pp][c] = __bfloat162float(__float2bfloat16(hv));
  }
  __syncthreads();
  for (int pp = 0; pp < 4; ++pp) {
    float a = 0.f;
    for (int k = 0; k < C; ++k) a += hn[pp][k] * bf2f(w1[k * HD + t]);
    float u = 0.7978845608028654f * (a + 0.044715f * a * a * a);
    float gl = 0.5f * a * (1.f + tanhf(u));
    g[pp][t] = __bfloat162float(__float2bfloat16(gl));
  }
  __syncthreads();
  {
    int pp = t / C, c = t % C;
    float a = 0.f;
    for (int k = 0; k < HD; ++k) a += g[pp][k] * bf2f(w2[k * C + c]);
    int p = p0 + pp;
    out[p * C + c] = __float2bfloat16(bf2f(x[p * C + c]) + a);
  }
}

extern "C" void kernel_launch(void* const* d_in, const int* in_sizes, int n_in,
                              void* d_out, int out_size, void* d_ws, size_t ws_size,
                              hipStream_t stream) {
  naive_kernel<<<H * W / 4, 384, 0, stream>>>((const uint16_t*)d_in[0], (const uint16_t*)d_in[1],
      (const uint16_t*)d_in[2], (const uint16_t*)d_in[3], (__hip_bfloat16*)d_out);
}
```

```cpp
#include <hip/hip_runtime.h>
#include <hip/hip_bf16.h>

static __device__ const unsigned cnx_tab[6656] __attribute__((aligned(256))) = {
  0x0,0x0,0x0,0x0,0x8,0x10,0xd0,0xd0,0x10,0x8,0x1a0,0x1a0,0x18,0x18,0x270,0x270,
  0x40,0xc0,0x340,0x340,0x48,0xd0,0x410,0x410,0x50,0xc8,0x4e0,0x4e0,0x58,0xd8,0x5b0,0x5b0,
  0x80,0x180,0x680,0xb60,0x88,0x190,0x750,0xc30,0x90,0x188,0x820,0xd00,0x98,0x198,0x8f0,0xdd0,
  0xc0,0x240,0x9c0,0xea0,0xc8,0x250,0xa90,0xf70,0xd0,0x248,0xb60,0x1040,0xd8,0x258,0xc30,0x1110,
  0x20,0x20,0xd00,0x2d80,0x28,0x30,0xdd0,0x2e50,0x30,0x28,0xea0,0x2f20,0x38,0x38,0xf70,0x2ff0,
  0x60,0xe0,0x1040,0x30c0,0x68,0xf0,0x1110,0x3190,0x70,0xe8,0x11e0,0x3260,0x78,0xf8,0x12b0,0x3330,
  0xa0,0x1a0,0x1380,0x38e0,0xa8,0x1b0,0x1450,0x39b0,0xb0,0x1a8,0x1520,0x3a80,0xb8,0x1b8,0x15f0,0x3b50,
  0xe0,0x260,0x16c0,0x3c20,0xe8,0x270,0x1790,0x3cf0,0xf0,0x268,0x1860,0x3dc0,0xf8,0x278,0x1930,0x3e90,
  0x120,0x320,0x8,0x5b00,0x128,0x330,0xd8,0x5bd0,0x130,0x328,0x1a8,0x5ca0,0x138,0x338,0x278,0x5d70,
  0x160,0x3e0,0x348,0x5e40,0x168,0x3f0,0x418,0x5f10,0x170,0x3e8,0x4e8,0x5fe0,0x178,0x3f8,0x5b8,0x60b0,
  0x1a0,0x4a0,0x688,0x6660,0x1a8,0x4b0,0x758,0x6730,0x1b0,0x4a8,0x828,0x6800,0x1b8,0x4b8,0x8f8,0x68d0,
  0x1e0,0x560,0x9c8,0x69a0,0x1e8,0x570,0xa98,0x6a70,0x1f0,0x568,0xb68,0x6b40,0x1f8,0x578,0xc38,0x6c10,
  0x100,0x300,0xd08,0x8880,0x108,0x310,0xdd8,0x8950,0x110,0x308,0xea8,0x8a20,0x118,0x318,0xf78,0x8af0,
  0x140,0x3c0,0x1048,0x8bc0,0x148,0x3d0,0x1118,0x8c90,0x150,0x3c8,0x11e8,0x8d60,0x158,0x3d8,0x12b8,0x8e30,
  0x180,0x480,0x1388,0x93e0,0x188,0x490,0x1458,0x94b0,0x190,0x488,0x1528,0x9580,0x198,0x498,0x15f8,0x9650,
  0x1c0,0x540,0x16c8,0x9720,0x1c8,0x550,0x1798,0x97f0,0x1d0,0x548,0x1868,0x98c0,0x1d8,0x558,0x1938,0x9990,
  0x0,0x0,0x0,0x0,0xffffff30,0x10,0x10,0x1e20,0xfffffe60,0x60,0x20,0x3c40,0xfffffd90,0x70,0x30,0x5a60,
  0xfffffcc0,0xc0,0x300,0x8,0xfffffbf0,0xd0,0x310,0x1e28,0xfffffb20,0x120,0x320,0x3c48,0xfffffa50,0x130,0x330,0x5a68,
  0xfffff980,0x180,0x600,0x10,0xfffff8b0,0x190,0x610,0x1e30,0xfffff7e0,0x1e0,0x620,0x3c50,0xfffff710,0x1f0,0x630,0x5a70,
  0xfffff640,0x240,0x900,0x18,0xfffff570,0x250,0x910,0x1e38,0xfffff4a0,0x240,0x920,0x3c58,0xfffff3d0,0x250,0x930,0x5a78,
  0x1a00,0xc0,0xc20,0x20,0x1930,0xd0,0xc30,0x1e40,0x1860,0x120,0xc00,0x3c60,0x1790,0x130,0xc10,0x5a80,
  0x16c0,0x180,0xf20,0x7880,0x15f0,0x190,0xf30,0x96a0,0x1520,0x1e0,0xf00,0xb4c0,0x1450,0x1f0,0xf10,0xd2e0,
  0x1380,0x240,0x1220,0x7888,0x12b0,0x250,0x1230,0x96a8,0x11e0,0x2a0,0x1200,0xb4c8,0x1110,0x2b0,0x1210,0xd2e8,
  0x1040,0x300,0x1520,0x7890,0xf70,0x310,0x1530,0x96b0,0xea0,0x300,0x1500,0xb4d0,0xdd0,0x310,0x1510,0xd2f0,
  0x680,0x10,0x40,0x7898,0x5b0,0x20,0x50,0x96b8,0x4e0,0x70,0x60,0xb4d8,0x410,0x80,0x70,0xd2f8,
  0x340,0xd0,0x340,0x78a0,0x270,0xe0,0x350,0x96c0,0x1a0,0x130,0x360,0xb4e0,0xd0,0x140,0x370,0xd300,
  0x0,0x190,0x640,0xf100,0xffffff30,0x1a0,0x650,0x10f20,0xfffffe60,0x1f0,0x660,0x12d40,0xfffffd90,0x200,0x670,0x14b60,
  0xfffffcc0,0x250,0x940,0xf108,0xfffffbf0,0x260,0x950,0x10f28,0xfffffb20,0x250,0x960,0x12d48,0xfffffa50,0x260,0x970,0x14b68,
  0x2080,0xd0,0xc60,0xf110,0x1fb0,0xe0,0xc70,0x10f30,0x1ee0,0x130,0xc40,0x12d50,0x1e10,0x140,0xc50,0x14b70,
  0x1d40,0x190,0xf60,0xf118,0x1c70,0x1a0,0xf70,0x10f38,0x1ba0,0x1f0,0xf40,0x12d58,0x1ad0,0x200,0xf50,0x14b78,
  0x1a00,0x250,0x1260,0xf120,0x1930,0x260,0x1270,0x10f40,0x1860,0x2b0,0x1240,0x12d60,0x1790,0x2c0,0x1250,0x14b80,
  0x16c0,0x310,0x1560,0x28,0x15f0,0x320,0x1570,0x1e48,0x1520,0x310,0x1540,0x3c68,0x1450,0x320,0x1550,0x5a88,
  0x5040100,0x5040100,0x5040100,0xc0c0100,0x5040c0c,0x5040100,0x5040100,0x5040100,0xc0c0c0c,0x5040100,0x5040100,0x5040100,0xc0c0c0c,0x5040c0c,0x5040100,0x5040100,
  0xc0c0c0c,0xc0c0c0c,0x5040100,0x5040100,0xc0c0c0c,0xc0c0c0c,0x5040c0c,0x5040100,0xc0c0c0c,0xc0c0c0c,0xc0c0c0c,0x5040100,0xc0c0c0c,0xc0c0c0c,0xc0c0c0c,0x5040c0c,
  0x5040100,0x5040100,0x5040100,0xc0c0100,0x5040c0c,0x5040100,0x5040100,0x5040100,0xc0c0c0c,0x5040100,0x5040100,0x5040100,0xc0c0c0c,0x5040c0c,0x5040100,0x5040100,
  0xc0c0c0c,0xc0c0c0c,0x5040100,0x5040100,0xc0c0c0c,0xc0c0c0c,0x5040c0c,0x5040100,0xc0c0c0c,0xc0c0c0c,0xc0c0c0c,0x5040100,0xc0c0c0c,0xc0c0c0c,0xc0c0c0c,0x5040c0c,
  0x5040100,0x5040100,0x5040100,0xc0c0100,0x5040c0c,0x5040100,0x5040100,0x5040100,0xc0c0c0c,0x5040100,0x5040100,0x5040100,0xc0c0c0c,0x5040c0c,0x5040100,0x5040100,
  0xc0c0c0c,0xc0c0c0c,0x5040100,0x5040100,0xc0c0c0c,0xc0c0c0c,0x5040c0c,0x5040100,0xc0c0c0c,0xc0c0c0c,0xc0c0c0c,0x5040100,0xc0c0c0c,0xc0c0c0c,0xc0c0c0c,0x5040c0c,
  0x5040100,0x5040100,0x5040100,0xc0c0100,0x5040c0c,0x5040100,0x5040100,0x5040100,0xc0c0c0c,0x5040100,0x5040100,0x5040100,0xc0c0c0c,0x5040c0c,0x5040100,0x5040100,
  0xc0c0c0c,0xc0c0c0c,0x5040100,0x5040100,0xc0c0c0c,0xc0c0c0c,0x5040c0c,0x5040100,0xc0c0c0c,0xc0c0c0c,0xc0c0c0c,0x5040100,0xc0c0c0c,0xc0c0c0c,0xc0c0c0c,0x5040c0c,
  0xc0c0c0c,0xc0c0c0c,0xc0c0c0c,0xc0c0c0c,0xc0c0c0c,0xc0c0c0c,0xc0c0c0c,0xc0c0c0c,0xc0c0100,0xc0c0c0c,0xc0c0c0c,0xc0c0c0c,0x5040100,0xc0c0c0c,0xc0c0c0c,0xc0c0c0c,
  0x5040100,0xc0c0100,0xc0c0c0c,0xc0c0c0c,0x5040100,0x5040100,0xc0c0c0c,0xc0c0c0c,0x5040100,0x5040100,0xc0c0100,0xc0c0c0c,0x5040100,0x5040100,0x5040100,0xc0c0c0c,
  0xc0c0c0c,0xc0c0c0c,0xc0c0c0c,0xc0c0c0c,0xc0c0c0c,0xc0c0c0c,0xc0c0c0c,0xc0c0c0c,0xc0c0100,0xc0c0c0c,0xc0c0c0c,0xc0c0c0c,0x5040100,0xc0c0c0c,0xc0c0c0c,0xc0c0c0c,
  0x5040100,0xc0c0100,0xc0c0c0c,0xc0c0c0c,0x5040100,0x5040100,0xc0c0c0c,0xc0c0c0c,0x5040100,0x5040100,0xc0c0100,0xc0c0c0c,0x5040100,0x5040100,0x5040100,0xc0c0c0c,
  0xc0c0c0c,0xc0c0c0c,0xc0c0c0c,0xc0c0c0c,0xc0c0c0c,0xc0c0c0c,0xc0c0c0c,0xc0c0c0c,0xc0c0100,0xc0c0c0c,0xc0c0c0c,0xc0c0c0c,0x5040100,0xc0c0c0c,0xc0c0c0c,0xc0c0c0c,
  0x5040100,0xc0c0100,0xc0c0c0c,0xc0c0c0c,0x5040100,0x5040100,0xc0c0c0c,0xc0c0c0c,0x5040100,0x5040100,0xc0c0100,0xc0c0c0c,0x5040100,0x5040100,0x5040100,0xc0c0c0c,
  0xc0c0c0c,0xc0c0c0c,0xc0c0c0c,0xc0c0c0c,0xc0c0c0c,0xc0c0c0c,0xc0c0c0c,0xc0c0c0c,0xc0c0100,0xc0c0c0c,0xc0c0c0c,0xc0c0c0c,0x5040100,0xc0c0c0c,0xc0c0c0c,0xc0c0c0c,
  0x5040100,0xc0c0100,0xc0c0c0c,0xc0c0c0c,0x5040100,0x5040100,0xc0c0c0c,0xc0c0c0c,0x5040100,0x5040100,0xc0c0100,0xc0c0c0c,0x5040100,0x5040100,0x5040100,0xc0c0c0c,
  0x7060302,0x7060302,0x7060302,0xc0c0302,0x7060c0c,0x7060302,0x7060302,0x7060302,0xc0c0c0c,0x7060302,0x7060302,0x7060302,0xc0c0c0c,0x7060c0c,0x7060302,0x7060302,
  0xc0c0c0c,0xc0c0c0c,0x7060302,0x7060302,0xc0c0c0c,0xc0c0c0c,0x7060c0c,0x7060302,0xc0c0c0c,0xc0c0c0c,0xc0c0c0c,0x7060302,0xc0c0c0c,0xc0c0c0c,0xc0c0c0c,0x7060c0c,
  0x7060302,0x7060302,0x7060302,0xc0c0302,0x7060c0c,0x7060302,0x7060302,0x7060302,0xc0c0c0c,0x7060302,0x7060302,0x7060302,0xc0c0c0c,0x7060c0c,0x7060302,0x7060302,
  0xc0c0c0c,0xc0c0c0c,0x7060302,0x7060302,0xc0c0c0c,0xc0c0c0c,0x7060c0c,0x7060302,0xc0c0c0c,0xc0c0c0c,0xc0c0c0c,0x7060302,0xc0c0c0c,0xc0c0c0c,0xc0c0c0c,0x7060c0c,
  0x7060302,0x7060302,0x7060302,0xc0c0302,0x7060c0c,0x7060302,0x7060302,0x7060302,0xc0c0c0c,0x7060302,0x7060302,0x7060302,0xc0c0c0c,0x7060c0c,0x7060302,0x7060302,
  0xc0c0c0c,0xc0c0c0c,0x7060302,0x7060302,0xc0c0c0c,0xc0c0c0c,0x7060c0c,0x7060302,0xc0c0c0c,0xc0c0c0c,0xc0c0c0c,0x7060302,0xc0c0c0c,0xc0c0c0c,0xc0c0c0c,0x7060c0c,
  0x7060302,0x7060302,0x7060302,0xc0c0302,0x7060c0c,0x7060302,0x7060302,0x7060302,0xc0c0c0c,0x7060302,0x7060302,0x7060302,0xc0c0c0c,0x7060c0c,0x7060302,0x7060302,
  0xc0c0c0c,0xc0c0c0c,0x7060302,0x7060302,0xc0c0c0c,0xc0c0c0c,0x7060c0c,0x7060302,0xc0c0c0c,0xc0c0c0c,0xc0c0c0c,0x7060302,0xc0c0c0c,0xc0c0c0c,0xc0c0c0c,0x7060c0c,
  0xc0c0c0c,0xc0c0c0c,0xc0c0c0c,0xc0c0c0c,0xc0c0c0c,0xc0c0c0c,0xc0c0c0c,0xc0c0c0c,0xc0c0302,0xc0c0c0c,0xc0c0c0c,0xc0c0c0c,0x7060302,0xc0c0c0c,0xc0c0c0c,0xc0c0c0c,
  0x7060302,0xc0c0302,0xc0c0c0c,0xc0c0c0c,0x7060302,0x7060302,0xc0c0c0c,0xc0c0c0c,0x7060302,0x7060302,0xc0c0302,0xc0c0c0c,0x7060302,0x7060302,0x7060302,0xc0c0c0c,
  0xc0c0c0c,0xc0c0c0c,0xc0c0c0c,0xc0c0c0c,0xc0c0c0c,0xc0c0c0c,0xc0c0c0c,0xc0c0c0c,0xc0c0302,0xc0c0c0c,0xc0c0c0c,0xc0c0c0c,0x7060302,0xc0c0c0c,0xc0c0c0c,0xc0c0c0c,
  0x7060302,0xc0c0302,0xc0c0c0c,0xc0c0c0c,0x7060302,0x7060302,0xc0c0c0c,0xc0c0c0c,0x7060302,0x7060302,0xc0c0302,0xc0c0c0c,0x7060302,0x7060302,0x7060302,0xc0c0c0c,
  0xc0c0c0c,0xc0c0c0c,0xc0c0c0c,0xc0c0c0c,0xc0c0c0c,0xc0c0c0c,0xc0c0c0c,0xc0c0c0c,0xc0c0302,0xc0c0c0c,0xc0c0c0c,0xc0c0c0c,0x7060302,0xc0c0c0c,0xc0c0c0c,0xc0c0c0c,
  0x7060302,0xc0c0302,0xc0c0c0c,0xc0c0c0c,0x7060302,0x7060302,0xc0c0c0c,0xc0c0c0c,0x7060302,0x7060302,0xc0c0302,0xc0c0c0c,0x7060302,0x7060302,0x7060302,0xc0c0c0c,
  0xc0c0c0c,0xc0c0c0c,0xc0c0c0c,0xc0c0c0c,0xc0c0c0c,0xc0c0c0c,0xc0c0c0c,0xc0c0c0c,0xc0c0302,0xc0c0c0c,0xc0c0c0c,0xc0c0c0c,0x7060302,0xc0c0c0c,0xc0c0c0c,0xc0c0c0c,
  0x7060302,0xc0c0302,0xc0c0c0c,0xc0c0c0c,0x7060302,0x7060302,0xc0c0c0c,0xc0c0c0c,0x7060302,0x7060302,0xc0c0302,0xc0c0c0c,0x7060302,0x7060302,0x7060302,0xc0c0c0c,
  0x3f80,0x0,0x0,0x0,0x3f800000,0x0,0x0,0x0,0x0,0x3f80,0x0,0x0,0x0,0x3f800000,0x0,0x0,
  0x0,0x0,0x0,0x0,0x0,0x0,0x0,0x0,0x0,0x0,0x0,0x0,0x0,0x0,0x0,0x0,
  0x0,0x0,0x3f80,0x0,0x0,0x0,0x3f800000,0x0,0x0,0x0,0x0,0x3f80,0x0,0x0,0x0,0x3f800000,
  0x0,0x0,0x0,0x0,0x0,0x0,0x0,0x0,0x0,0x0,0x0,0x0,0x0,0x0,0x0,0x0,
  0x0,0x0,0x0,0x0,0x0,0x0,0x0,0x0,0x0,0x0,0x0,0x0,0x0,0x0,0x0,0x0,
  0x0,0x0,0x0,0x0,0x0,0x0,0x0,0x0,0x0,0x0,0x0,0x0,0x0,0x0,0x0,0x0,
  0x0,0x0,0x0,0x0,0x0,0x0,0x0,0x0,0x0,0x0,0x0,0x0,0x0,0x0,0x0,0x0,
  0x0,0x0,0x0,0x0,0x0,0x0,0x0,0x0,0x0,0x0,0x0,0x0,0x0,0x0,0x0,0x0,
  0x0,0x0,0x0,0x0,0x0,0x0,0x0,0x0,0x0,0x0,0x0,0x0,0x0,0x0,0x0,0x0,
  0x3f80,0x0,0x0,0x0,0x3f800000,0x0,0x0,0x0,0x0,0x3f80,0x0,0x0,0x0,0x3f800000,0x0,0x0,
  0x0,0x0,0x0,0x0,0x0,0x0,0x0,0x0,0x0,0x0,0x0,0x0,0x0,0x0,0x0,0x0,
  0x0,0x0,0x3f80,0x0,0x0,0x0,0x3f800000,0x0,0x0,0x0,0x0,0x3f80,0x0,0x0,0x0,0x3f800000,
  0x0,0x0,0x0,0x0,0x0,0x0,0x0,0x0,0x0,0x0,0x0,0x0,0x0,0x0,0x0,0x0,
  0x0,0x0,0x0,0x0,0x0,0x0,0x0,0x0,0x0,0x0,0x0,0x0,0x0,0x0,0x0,0x0,
  0x0,0x0,0x0,0x0,0x0,0x0,0x0,0x0,0x0,0x0,0x0,0x0,0x0,0x0,0x0,0x0,
  0x0,0x0,0x0,0x0,0x0,0x0,0x0,0x0,0x0,0x0,0x0,0x0,0x0,0x0,0x0,0x0,
  0x0,0x0,0x0,0x0,0x0,0x0,0x0,0x0,0x0,0x0,0x0,0x0,0x0,0x0,0x0,0x0,
  0x0,0x0,0x0,0x0,0x0,0x0,0x0,0x0,0x0,0x0,0x0,0x0,0x0,0x0,0x0,0x0,
  0x0,0x0,0x0,0x0,0x0,0x0,0x0,0x0,0x0,0x0,0x0,0x0,0x0,0x0,0x0,0x0,
  0x0,0x0,0x0,0x0,0x0,0x0,0x0,0x0,0x0,0x0,0x0,0x0,0x0,0x0,0x0,0x0,
  0x3f80,0x0,0x0,0x0,0x3f800000,0x0,0x0,0x0,0x0,0x3f80,0x0,0x0,0x0,0x3f800000,0x0,0x0,
  0x0,0x0,0x0,0x0,0x0,0x0,0x0,0x0,0x0,0x0,0x0,0x0,0x0,0x0,0x0,0x0,
  0x0,0x0,0x3f80,0x0,0x0,0x0,0x3f800000,0x0,0x0,0x0,0x0,0x3f80,0x0,0x0,0x0,0x3f800000,
  0x0,0x0,0x0,0x0,0x0,0x0,0x0,0x0,0x0,0x0,0x0,0x0,0x0,0x0,0x0,0x0,
  0x0,0x0,0x0,0x0,0x0,0x0,0x0,0x0,0x0,0x0,0x0,0x0,0x0,0x0,0x0,0x0,
  0x0,0x0,0x0,0x0,0x0,0x0,0x0,0x0,0x0,0x0,0x0,0x0,0x0,0x0,0x0,0x0,
  0x0,0x0,0x0,0x0,0x0,0x0,0x0,0x0,0x0,0x0,0x0,0x0,0x0,0x0,0x0,0x0,
  0x0,0x0,0x0,0x0,0x0,0x0,0x0,0x0,0x0,0x0,0x0,0x0,0x0,0x0,0x0,0x0,
  0x0,0x0,0x0,0x0,0x0,0x0,0x0,0x0,0x0,0x0,0x0,0x0,0x0,0x0,0x0,0x0,
  0x3f80,0x0,0x0,0x0,0x3f800000,0x0,0x0,0x0,0x0,0x3f80,0x0,0x0,0x0,0x3f800000,0x0,0x0,
  0x0,0x0,0x0,0x0,0x0,0x0,0x0,0x0,0x0,0x0,0x0,0x0,0x0,0x0,0x0,0x0,
  0x0,0x0,0x3f80,0x0,0x0,0x0,0x3f800000,0x0,0x0,0x0,0x0,0x3f80,0x0,0x0,0x0,0x3f800000,
  0x0,0x3080,0x0,0x0,0xc0,0x3150,0x8,0x8,0x190,0x3220,0x10,0x10,0x250,0x32f0,0x18,0x18,
  0x320,0x3080,0x40,0xc0,0x3e0,0x3150,0x48,0xc8,0x4b0,0x3220,0x50,0xd0,0x570,0x32f0,0x58,0xd8,
  0x600,0x3080,0x80,0x180,0x6c0,0x3150,0x88,0x188,0x790,0x3220,0x90,0x190,0x850,0x32f0,0x98,0x198,
  0x920,0x3080,0xc0,0x240,0x9e0,0x3150,0xc8,0x248,0xab0,0x3220,0xd0,0x250,0xb70,0x32f0,0xd8,0x258,
  0xc00,0x3088,0x120,0x320,0xcc0,0x3158,0x128,0x328,0xd90,0x3228,0x130,0x330,0xe50,0x32f8,0x138,0x338,
  0xf20,0x3088,0x160,0x3e0,0xfe0,0x3158,0x168,0x3e8,0x10b0,0x3228,0x170,0x3f0,0x1170,0x32f8,0x178,0x3f8,
  0x1200,0x3088,0x1a0,0x4a0,0x12c0,0x3158,0x1a8,0x4a8,0x1390,0x3228,0x1b0,0x4b0,0x1450,0x32f8,0x1b8,0x4b8,
  0x1520,0x3088,0x1e0,0x560,0x15e0,0x3158,0x1e8,0x568,0x16b0,0x3228,0x1f0,0x570,0x1770,0x32f8,0x1f8,0x578,
  0x10,0x3090,0x400,0x600,0xd0,0x3160,0x408,0x608,0x180,0x3230,0x410,0x610,0x240,0x3300,0x418,0x618,
  0x330,0x3090,0x440,0x6c0,0x3f0,0x3160,0x448,0x6c8,0x4a0,0x3230,0x450,0x6d0,0x560,0x3300,0x458,0x6d8,
  0x610,0x3090,0x480,0x780,0x6d0,0x3160,0x488,0x788,0x780,0x3230,0x490,0x790,0x840,0x3300,0x498,0x798,
  0x930,0x3090,0x4c0,0x840,0x9f0,0x3160,0x4c8,0x848,0xaa0,0x3230,0x4d0,0x850,0xb60,0x3300,0x4d8,0x858,
  0xc10,0x3098,0x520,0x920,0xcd0,0x3168,0x528,0x928,0xd80,0x3238,0x530,0x930,0xe40,0x3308,0x538,0x938,
  0xf30,0x3098,0x560,0x9e0,0xff0,0x3168,0x568,0x9e8,0x10a0,0x3238,0x570,0x9f0,0x1160,0x3308,0x578,0x9f8,
  0x1210,0x3098,0x5a0,0xaa0,0x12d0,0x3168,0x5a8,0xaa8,0x1380,0x3238,0x5b0,0xab0,0x1440,0x3308,0x5b8,0xab8,
  0x1530,0x3098,0x5e0,0xb60,0x15f0,0x3168,0x5e8,0xb68,0x16a0,0x3238,0x5f0,0xb70,0x1760,0x3308,0x5f8,0xb78,
  0x0,0x420,0x810,0xa980,0x10,0x430,0x800,0xa990,0x20,0x400,0x830,0xa9a0,0x30,0x410,0x820,0xa9b0,
  0x40,0x460,0x850,0xa9c0,0x50,0x470,0x840,0xa9d0,0x60,0x440,0x870,0xa9e0,0x70,0x450,0x860,0xa9f0,
  0x80,0x4b0,0x890,0xaa00,0x90,0x4a0,0x880,0xaa10,0xa0,0x490,0x8b0,0xaa20,0xb0,0x480,0x8a0,0xaa30,
  0xc0,0x4f0,0x8d0,0xaa40,0xd0,0x4e0,0x8c0,0xaa50,0xe0,0x4d0,0x8f0,0xaa60,0xf0,0x4c0,0x8e0,0xaa70,
  0x100,0x530,0x920,0xaa80,0x110,0x520,0x930,0xaa90,0x120,0x510,0x900,0xaaa0,0x130,0x500,0x910,0xaab0,
  0x140,0x570,0x960,0xaac0,0x150,0x560,0x970,0xaad0,0x160,0x550,0x940,0xaae0,0x170,0x540,0x950,0xaaf0,
  0x190,0x5b0,0x9a0,0xab10,0x180,0x5a0,0x9b0,0xab00,0x1b0,0x590,0x980,0xab30,0x1a0,0x580,0x990,0xab20,
  0x1d0,0x5f0,0x9e0,0xab50,0x1c0,0x5e0,0x9f0,0xab40,0x1f0,0x5d0,0x9c0,0xab70,0x1e0,0x5c0,0x9d0,0xab60,
  0x210,0x600,0xa20,0xab90,0x200,0x610,0xa30,0xab80,0x230,0x620,0xa00,0xabb0,0x220,0x630,0xa10,0xaba0,
  0x250,0x640,0xa60,0xabd0,0x240,0x650,0xa70,0xabc0,0x270,0x660,0xa40,0xabf0,0x260,0x670,0xa50,0xabe0,
  0x290,0x680,0xa830,0xac10,0x280,0x690,0xa820,0xac00,0x2b0,0x6a0,0xa810,0xac30,0x2a0,0x6b0,0xa800,0xac20,
  0x2d0,0x6c0,0xa870,0xac50,0x2c0,0x6d0,0xa860,0xac40,0x2f0,0x6e0,0xa850,0xac70,0x2e0,0x6f0,0xa840,0xac60,
  0x320,0x700,0xa8b0,0xaca0,0x330,0x710,0xa8a0,0xacb0,0x300,0x720,0xa890,0xac80,0x310,0x730,0xa880,0xac90,
  0x360,0x740,0xa8f0,0xace0,0x370,0x750,0xa8e0,0xacf0,0x340,0x760,0xa8d0,0xacc0,0x350,0x770,0xa8c0,0xacd0,
  0x3a0,0x790,0xa930,0xad20,0x3b0,0x780,0xa920,0xad30,0x380,0x7b0,0xa910,0xad00,0x390,0x7a0,0xa900,0xad10,
  0x3e0,0x7d0,0xa970,0xad60,0x3f0,0x7c0,0xa960,0xad70,0x3c0,0x7f0,0xa950,0xad40,0x3d0,0x7e0,0xa940,0xad50,
  0x15300,0x15720,0x1f890,0x1fc80,0x15310,0x15730,0x1f880,0x1fc90,0x15320,0x15700,0x1f8b0,0x1fca0,0x15330,0x15710,0x1f8a0,0x1fcb0,
  0x15340,0x15760,0x1f8d0,0x1fcc0,0x15350,0x15770,0x1f8c0,0x1fcd0,0x15360,0x15740,0x1f8f0,0x1fce0,0x15370,0x15750,0x1f8e0,0x1fcf0,
  0x15380,0x157b0,0x1f910,0x1fd00,0x15390,0x157a0,0x1f900,0x1fd10,0x153a0,0x15790,0x1f930,0x1fd20,0x153b0,0x15780,0x1f920,0x1fd30,
  0x153c0,0x157f0,0x1f950,0x1fd40,0x153d0,0x157e0,0x1f940,0x1fd50,0x153e0,0x157d0,0x1f970,0x1fd60,0x153f0,0x157c0,0x1f960,0x1fd70,
  0x15400,0x15830,0x1f9a0,0x1fd80,0x15410,0x15820,0x1f9b0,0x1fd90,0x15420,0x15810,0x1f980,0x1fda0,0x15430,0x15800,0x1f990,0x1fdb0,
  0x15440,0x15870,0x1f9e0,0x1fdc0,0x15450,0x15860,0x1f9f0,0x1fdd0,0x15460,0x15850,0x1f9c0,0x1fde0,0x15470,0x15840,0x1f9d0,0x1fdf0,
  0x15490,0x158b0,0x1fa20,0x1fe10,0x15480,0x158a0,0x1fa30,0x1fe00,0x154b0,0x15890,0x1fa00,0x1fe30,0x154a0,0x15880,0x1fa10,0x1fe20,
  0x154d0,0x158f0,0x1fa60,0x1fe50,0x154c0,0x158e0,0x1fa70,0x1fe40,0x154f0,0x158d0,0x1fa40,0x1fe70,0x154e0,0x158c0,0x1fa50,0x1fe60,
  0x15510,0x15900,0x1faa0,0x1fe90,0x15500,0x15910,0x1fab0,0x1fe80,0x15530,0x15920,0x1fa80,0x1feb0,0x15520,0x15930,0x1fa90,0x1fea0,
  0x15550,0x15940,0x1fae0,0x1fed0,0x15540,0x15950,0x1faf0,0x1fec0,0x15570,0x15960,0x1fac0,0x1fef0,0x15560,0x15970,0x1fad0,0x1fee0,
  0x15590,0x15980,0x1fb30,0x1ff10,0x15580,0x15990,0x1fb20,0x1ff00,0x155b0,0x159a0,0x1fb10,0x1ff30,0x155a0,0x159b0,0x1fb00,0x1ff20,
  0x155d0,0x159c0,0x1fb70,0x1ff50,0x155c0,0x159d0,0x1fb60,0x1ff40,0x155f0,0x159e0,0x1fb50,0x1ff70,0x155e0,0x159f0,0x1fb40,0x1ff60,
  0x15620,0x15a00,0x1fbb0,0x1ffa0,0x15630,0x15a10,0x1fba0,0x1ffb0,0x15600,0x15a20,0x1fb90,0x1ff80,0x15610,0x15a30,0x1fb80,0x1ff90,
  0x15660,0x15a40,0x1fbf0,0x1ffe0,0x15670,0x15a50,0x1fbe0,0x1fff0,0x15640,0x15a60,0x1fbd0,0x1ffc0,0x15650,0x15a70,0x1fbc0,0x1ffd0,
  0x156a0,0x1f810,0x1fc30,0x20020,0x156b0,0x1f800,0x1fc20,0x20030,0x15680,0x1f830,0x1fc10,0x20000,0x15690,0x1f820,0x1fc00,0x20010,
  0x156e0,0x1f850,0x1fc70,0x20060,0x156f0,0x1f840,0x1fc60,0x20070,0x156c0,0x1f870,0x1fc50,0x20040,0x156d0,0x1f860,0x1fc40,0x20050,
  0x2a600,0x2aa20,0x34b90,0x34f80,0x2a610,0x2aa30,0x34b80,0x34f90,0x2a620,0x2aa00,0x34bb0,0x34fa0,0x2a630,0x2aa10,0x34ba0,0x34fb0,
  0x2a640,0x2aa60,0x34bd0,0x34fc0,0x2a650,0x2aa70,0x34bc0,0x34fd0,0x2a660,0x2aa40,0x34bf0,0x34fe0,0x2a670,0x2aa50,0x34be0,0x34ff0,
  0x2a680,0x34830,0x34c10,0x35000,0x2a690,0x34820,0x34c00,0x35010,0x2a6a0,0x34810,0x34c30,0x35020,0x2a6b0,0x34800,0x34c20,0x35030,
  0x2a6c0,0x34870,0x34c50,0x35040,0x2a6d0,0x34860,0x34c40,0x35050,0x2a6e0,0x34850,0x34c70,0x35060,0x2a6f0,0x34840,0x34c60,0x35070,
  0x2a700,0x348b0,0x34ca0,0x35080,0x2a710,0x348a0,0x34cb0,0x35090,0x2a720,0x34890,0x34c80,0x350a0,0x2a730,0x34880,0x34c90,0x350b0,
  0x2a740,0x348f0,0x34ce0,0x350c0,0x2a750,0x348e0,0x34cf0,0x350d0,0x2a760,0x348d0,0x34cc0,0x350e0,0x2a770,0x348c0,0x34cd0,0x350f0,
  0x2a790,0x34930,0x34d20,0x35110,0x2a780,0x34920,0x34d30,0x35100,0x2a7b0,0x34910,0x34d00,0x35130,0x2a7a0,0x34900,0x34d10,0x35120,
  0x2a7d0,0x34970,0x34d60,0x35150,0x2a7c0,0x34960,0x34d70,0x35140,0x2a7f0,0x34950,0x34d40,0x35170,0x2a7e0,0x34940,0x34d50,0x35160,
  0x2a810,0x34980,0x34da0,0x35190,0x2a800,0x34990,0x34db0,0x35180,0x2a830,0x349a0,0x34d80,0x351b0,0x2a820,0x349b0,0x34d90,0x351a0,
  0x2a850,0x349c0,0x34de0,0x351d0,0x2a840,0x349d0,0x34df0,0x351c0,0x2a870,0x349e0,0x34dc0,0x351f0,0x2a860,0x349f0,0x34dd0,0x351e0,
  0x2a890,0x34a00,0x34e30,0x35210,0x2a880,0x34a10,0x34e20,0x35200,0x2a8b0,0x34a20,0x34e10,0x35230,0x2a8a0,0x34a30,0x34e00,0x35220,
  0x2a8d0,0x34a40,0x34e70,0x35250,0x2a8c0,0x34a50,0x34e60,0x35240,0x2a8f0,0x34a60,0x34e50,0x35270,0x2a8e0,0x34a70,0x34e40,0x35260,
  0x2a920,0x34a80,0x34eb0,0x3f020,0x2a930,0x34a90,0x34ea0,0x3f030,0x2a900,0x34aa0,0x34e90,0x3f000,0x2a910,0x34ab0,0x34e80,0x3f010,
  0x2a960,0x34ac0,0x34ef0,0x3f060,0x2a970,0x34ad0,0x34ee0,0x3f070,0x2a940,0x34ae0,0x34ed0,0x3f040,0x2a950,0x34af0,0x34ec0,0x3f050,
  0x2a9a0,0x34b10,0x34f30,0x3f0a0,0x2a9b0,0x34b00,0x34f20,0x3f0b0,0x2a980,0x34b30,0x34f10,0x3f080,0x2a990,0x34b20,0x34f00,0x3f090,
  0x2a9e0,0x34b50,0x34f70,0x3f0e0,0x2a9f0,0x34b40,0x34f60,0x3f0f0,0x2a9c0,0x34b70,0x34f50,0x3f0c0,0x2a9d0,0x34b60,0x34f40,0x3f0d0,
  0x3f900,0x49aa0,0x49e90,0x54000,0x3f910,0x49ab0,0x49e80,0x54010,0x3f920,0x49a80,0x49eb0,0x54020,0x3f930,0x49a90,0x49ea0,0x54030,
  0x3f940,0x49ae0,0x49ed0,0x54040,0x3f950,0x49af0,0x49ec0,0x54050,0x3f960,0x49ac0,0x49ef0,0x54060,0x3f970,0x49ad0,0x49ee0,0x54070,
  0x3f980,0x49b30,0x49f10,0x54080,0x3f990,0x49b20,0x49f00,0x54090,0x3f9a0,0x49b10,0x49f30,0x540a0,0x3f9b0,0x49b00,0x49f20,0x540b0,
  0x3f9c0,0x49b70,0x49f50,0x540c0,0x3f9d0,0x49b60,0x49f40,0x540d0,0x3f9e0,0x49b50,0x49f70,0x540e0,0x3f9f0,0x49b40,0x49f60,0x540f0,
  0x3fa00,0x49bb0,0x49fa0,0x54100,0x3fa10,0x49ba0,0x49fb0,0x54110,0x3fa20,0x49b90,0x49f80,0x54120,0x3fa30,0x49b80,0x49f90,0x54130,
  0x3fa40,0x49bf0,0x49fe0,0x54140,0x3fa50,0x49be0,0x49ff0,0x54150,0x3fa60,0x49bd0,0x49fc0,0x54160,0x3fa70,0x49bc0,0x49fd0,0x54170,
  0x49810,0x49c30,0x4a020,0x54190,0x49800,0x49c20,0x4a030,0x54180,0x49830,0x49c10,0x4a000,0x541b0,0x49820,0x49c00,0x4a010,0x541a0,
  0x49850,0x49c70,0x4a060,0x541d0,0x49840,0x49c60,0x4a070,0x541c0,0x49870,0x49c50,0x4a040,0x541f0,0x49860,0x49c40,0x4a050,0x541e0,
  0x49890,0x49c80,0x4a0a0,0x54210,0x49880,0x49c90,0x4a0b0,0x54200,0x498b0,0x49ca0,0x4a080,0x54230,0x498a0,0x49cb0,0x4a090,0x54220,
  0x498d0,0x49cc0,0x4a0e0,0x54250,0x498c0,0x49cd0,0x4a0f0,0x54240,0x498f0,0x49ce0,0x4a0c0,0x54270,0x498e0,0x49cf0,0x4a0d0,0x54260,
  0x49910,0x49d00,0x4a130,0x54290,0x49900,0x49d10,0x4a120,0x54280,0x49930,0x49d20,0x4a110,0x542b0,0x49920,0x49d30,0x4a100,0x542a0,
  0x49950,0x49d40,0x4a170,0x542d0,0x49940,0x49d50,0x4a160,0x542c0,0x49970,0x49d60,0x4a150,0x542f0,0x49960,0x49d70,0x4a140,0x542e0,
  0x499a0,0x49d80,0x4a1b0,0x54320,0x499b0,0x49d90,0x4a1a0,0x54330,0x49980,0x49da0,0x4a190,0x54300,0x49990,0x49db0,0x4a180,0x54310,
  0x499e0,0x49dc0,0x4a1f0,0x54360,0x499f0,0x49dd0,0x4a1e0,0x54370,0x499c0,0x49de0,0x4a1d0,0x54340,0x499d0,0x49df0,0x4a1c0,0x54350,
  0x49a20,0x49e10,0x4a230,0x543a0,0x49a30,0x49e00,0x4a220,0x543b0,0x49a00,0x49e30,0x4a210,0x54380,0x49a10,0x49e20,0x4a200,0x54390,
  0x49a60,0x49e50,0x4a270,0x543e0,0x49a70,0x49e40,0x4a260,0x543f0,0x49a40,0x49e70,0x4a250,0x543c0,0x49a50,0x49e60,0x4a240,0x543d0,
  0x5e980,0x5eda0,0x5f190,0x69300,0x5e990,0x5edb0,0x5f180,0x69310,0x5e9a0,0x5ed80,0x5f1b0,0x69320,0x5e9b0,0x5ed90,0x5f1a0,0x69330,
  0x5e9c0,0x5ede0,0x5f1d0,0x69340,0x5e9d0,0x5edf0,0x5f1c0,0x69350,0x5e9e0,0x5edc0,0x5f1f0,0x69360,0x5e9f0,0x5edd0,0x5f1e0,0x69370,
  0x5ea00,0x5ee30,0x5f210,0x69380,0x5ea10,0x5ee20,0x5f200,0x69390,0x5ea20,0x5ee10,0x5f230,0x693a0,0x5ea30,0x5ee00,0x5f220,0x693b0,
  0x5ea40,0x5ee70,0x5f250,0x693c0,0x5ea50,0x5ee60,0x5f240,0x693d0,0x5ea60,0x5ee50,0x5f270,0x693e0,0x5ea70,0x5ee40,0x5f260,0x693f0,
  0x5ea80,0x5eeb0,0x69020,0x69400,0x5ea90,0x5eea0,0x69030,0x69410,0x5eaa0,0x5ee90,0x69000,0x69420,0x5eab0,0x5ee80,0x69010,0x69430,
  0x5eac0,0x5eef0,0x69060,0x69440,0x5ead0,0x5eee0,0x69070,0x69450,0x5eae0,0x5eed0,0x69040,0x69460,0x5eaf0,0x5eec0,0x69050,0x69470,
  0x5eb10,0x5ef30,0x690a0,0x69490,0x5eb00,0x5ef20,0x690b0,0x69480,0x5eb30,0x5ef10,0x69080,0x694b0,0x5eb20,0x5ef00,0x69090,0x694a0,
  0x5eb50,0x5ef70,0x690e0,0x694d0,0x5eb40,0x5ef60,0x690f0,0x694c0,0x5eb70,0x5ef50,0x690c0,0x694f0,0x5eb60,0x5ef40,0x690d0,0x694e0,
  0x5eb90,0x5ef80,0x69120,0x69510,0x5eb80,0x5ef90,0x69130,0x69500,0x5ebb0,0x5efa0,0x69100,0x69530,0x5eba0,0x5efb0,0x69110,0x69520,
  0x5ebd0,0x5efc0,0x69160,0x69550,0x5ebc0,0x5efd0,0x69170,0x69540,0x5ebf0,0x5efe0,0x69140,0x69570,0x5ebe0,0x5eff0,0x69150,0x69560,
  0x5ec10,0x5f000,0x691b0,0x69590,0x5ec00,0x5f010,0x691a0,0x69580,0x5ec30,0x5f020,0x69190,0x695b0,0x5ec20,0x5f030,0x69180,0x695a0,
  0x5ec50,0x5f040,0x691f0,0x695d0,0x5ec40,0x5f050,0x691e0,0x695c0,0x5ec70,0x5f060,0x691d0,0x695f0,0x5ec60,0x5f070,0x691c0,0x695e0,
  0x5eca0,0x5f080,0x69230,0x69620,0x5ecb0,0x5f090,0x69220,0x69630,0x5ec80,0x5f0a0,0x69210,0x69600,0x5ec90,0x5f0b0,0x69200,0x69610,
  0x5ece0,0x5f0c0,0x69270,0x69660,0x5ecf0,0x5f0d0,0x69260,0x69670,0x5ecc0,0x5f0e0,0x69250,0x69640,0x5ecd0,0x5f0f0,0x69240,0x69650,
  0x5ed20,0x5f110,0x692b0,0x696a0,0x5ed30,0x5f100,0x692a0,0x696b0,0x5ed00,0x5f130,0x69290,0x69680,0x5ed10,0x5f120,0x69280,0x69690,
  0x5ed60,0x5f150,0x692f0,0x696e0,0x5ed70,0x5f140,0x692e0,0x696f0,0x5ed40,0x5f170,0x692d0,0x696c0,0x5ed50,0x5f160,0x692c0,0x696d0,
  0x73c80,0x740a0,0x7e210,0x7e600,0x73c90,0x740b0,0x7e200,0x7e610,0x73ca0,0x74080,0x7e230,0x7e620,0x73cb0,0x74090,0x7e220,0x7e630,
  0x73cc0,0x740e0,0x7e250,0x7e640,0x73cd0,0x740f0,0x7e240,0x7e650,0x73ce0,0x740c0,0x7e270,0x7e660,0x73cf0,0x740d0,0x7e260,0x7e670,
  0x73d00,0x74130,0x7e290,0x7e680,0x73d10,0x74120,0x7e280,0x7e690,0x73d20,0x74110,0x7e2b0,0x7e6a0,0x73d30,0x74100,0x7e2a0,0x7e6b0,
  0x73d40,0x74170,0x7e2d0,0x7e6c0,0x73d50,0x74160,0x7e2c0,0x7e6d0,0x73d60,0x74150,0x7e2f0,0x7e6e0,0x73d70,0x74140,0x7e2e0,0x7e6f0,
  0x73d80,0x741b0,0x7e320,0x7e700,0x73d90,0x741a0,0x7e330,0x7e710,0x73da0,0x74190,0x7e300,0x7e720,0x73db0,0x74180,0x7e310,0x7e730,
  0x73dc0,0x741f0,0x7e360,0x7e740,0x73dd0,0x741e0,0x7e370,0x7e750,0x73de0,0x741d0,0x7e340,0x7e760,0x73df0,0x741c0,0x7e350,0x7e770,
  0x73e10,0x74230,0x7e3a0,0x7e790,0x73e00,0x74220,0x7e3b0,0x7e780,0x73e30,0x74210,0x7e380,0x7e7b0,0x73e20,0x74200,0x7e390,0x7e7a0,
  0x73e50,0x74270,0x7e3e0,0x7e7d0,0x73e40,0x74260,0x7e3f0,0x7e7c0,0x73e70,0x74250,0x7e3c0,0x7e7f0,0x73e60,0x74240,0x7e3d0,0x7e7e0,
  0x73e90,0x7e000,0x7e420,0x7e810,0x73e80,0x7e010,0x7e430,0x7e800,0x73eb0,0x7e020,0x7e400,0x7e830,0x73ea0,0x7e030,0x7e410,0x7e820,
  0x73ed0,0x7e040,0x7e460,0x7e850,0x73ec0,0x7e050,0x7e470,0x7e840,0x73ef0,0x7e060,0x7e440,0x7e870,0x73ee0,0x7e070,0x7e450,0x7e860,
  0x73f10,0x7e080,0x7e4b0,0x7e890,0x73f00,0x7e090,0x7e4a0,0x7e880,0x73f30,0x7e0a0,0x7e490,0x7e8b0,0x73f20,0x7e0b0,0x7e480,0x7e8a0,
  0x73f50,0x7e0c0,0x7e4f0,0x7e8d0,0x73f40,0x7e0d0,0x7e4e0,0x7e8c0,0x73f70,0x7e0e0,0x7e4d0,0x7e8f0,0x73f60,0x7e0f0,0x7e4c0,0x7e8e0,
  0x73fa0,0x7e100,0x7e530,0x7e920,0x73fb0,0x7e110,0x7e520,0x7e930,0x73f80,0x7e120,0x7e510,0x7e900,0x73f90,0x7e130,0x7e500,0x7e910,
  0x73fe0,0x7e140,0x7e570,0x7e960,0x73ff0,0x7e150,0x7e560,0x7e970,0x73fc0,0x7e160,0x7e550,0x7e940,0x73fd0,0x7e170,0x7e540,0x7e950,
  0x74020,0x7e190,0x7e5b0,0x7e9a0,0x74030,0x7e180,0x7e5a0,0x7e9b0,0x74000,0x7e1b0,0x7e590,0x7e980,0x74010,0x7e1a0,0x7e580,0x7e990,
  0x74060,0x7e1d0,0x7e5f0,0x7e9e0,0x74070,0x7e1c0,0x7e5e0,0x7e9f0,0x74040,0x7e1f0,0x7e5d0,0x7e9c0,0x74050,0x7e1e0,0x7e5c0,0x7e9d0,
  0xada0,0xb190,0x0,0x0,0xadb0,0xb180,0x0,0x0,0xad80,0xb1b0,0x0,0x0,0xad90,0xb1a0,0x0,0x0,
  0xade0,0xb1d0,0x0,0x0,0xadf0,0xb1c0,0x0,0x0,0xadc0,0xb1f0,0x0,0x0,0xadd0,0xb1e0,0x0,0x0,
  0xae30,0xb210,0x0,0x0,0xae20,0xb200,0x0,0x0,0xae10,0xb230,0x0,0x0,0xae00,0xb220,0x0,0x0,
  0xae70,0xb250,0x0,0x0,0xae60,0xb240,0x0,0x0,0xae50,0xb270,0x0,0x0,0xae40,0xb260,0x0,0x0,
  0xaeb0,0x15020,0x0,0x0,0xaea0,0x15030,0x0,0x0,0xae90,0x15000,0x0,0x0,0xae80,0x15010,0x0,0x0,
  0xaef0,0x15060,0x0,0x0,0xaee0,0x15070,0x0,0x0,0xaed0,0x15040,0x0,0x0,0xaec0,0x15050,0x0,0x0,
  0xaf30,0x150a0,0x0,0x0,0xaf20,0x150b0,0x0,0x0,0xaf10,0x15080,0x0,0x0,0xaf00,0x15090,0x0,0x0,
  0xaf70,0x150e0,0x0,0x0,0xaf60,0x150f0,0x0,0x0,0xaf50,0x150c0,0x0,0x0,0xaf40,0x150d0,0x0,0x0,
  0xaf80,0x15120,0x0,0x0,0xaf90,0x15130,0x0,0x0,0xafa0,0x15100,0x0,0x0,0xafb0,0x15110,0x0,0x0,
  0xafc0,0x15160,0x0,0x0,0xafd0,0x15170,0x0,0x0,0xafe0,0x15140,0x0,0x0,0xaff0,0x15150,0x0,0x0,
  0xb000,0x151b0,0x0,0x0,0xb010,0x151a0,0x0,0x0,0xb020,0x15190,0x0,0x0,0xb030,0x15180,0x0,0x0,
  0xb040,0x151f0,0x0,0x0,0xb050,0x151e0,0x0,0x0,0xb060,0x151d0,0x0,0x0,0xb070,0x151c0,0x0,0x0,
  0xb080,0x15230,0x0,0x0,0xb090,0x15220,0x0,0x0,0xb0a0,0x15210,0x0,0x0,0xb0b0,0x15200,0x0,0x0,
  0xb0c0,0x15270,0x0,0x0,0xb0d0,0x15260,0x0,0x0,0xb0e0,0x15250,0x0,0x0,0xb0f0,0x15240,0x0,0x0,
  0xb110,0x152b0,0x0,0x0,0xb100,0x152a0,0x0,0x0,0xb130,0x15290,0x0,0x0,0xb120,0x15280,0x0,0x0,
  0xb150,0x152f0,0x0,0x0,0xb140,0x152e0,0x0,0x0,0xb170,0x152d0,0x0,0x0,0xb160,0x152c0,0x0,0x0,
  0x200a0,0x2a210,0x0,0x0,0x200b0,0x2a200,0x0,0x0,0x20080,0x2a230,0x0,0x0,0x20090,0x2a220,0x0,0x0,
  0x200e0,0x2a250,0x0,0x0,0x200f0,0x2a240,0x0,0x0,0x200c0,0x2a270,0x0,0x0,0x200d0,0x2a260,0x0,0x0,
  0x20130,0x2a290,0x0,0x0,0x20120,0x2a280,0x0,0x0,0x20110,0x2a2b0,0x0,0x0,0x20100,0x2a2a0,0x0,0x0,
  0x20170,0x2a2d0,0x0,0x0,0x20160,0x2a2c0,0x0,0x0,0x20150,0x2a2f0,0x0,0x0,0x20140,0x2a2e0,0x0,0x0,
  0x201b0,0x2a320,0x0,0x0,0x201a0,0x2a330,0x0,0x0,0x20190,0x2a300,0x0,0x0,0x20180,0x2a310,0x0,0x0,
  0x201f0,0x2a360,0x0,0x0,0x201e0,0x2a370,0x0,0x0,0x201d0,0x2a340,0x0,0x0,0x201c0,0x2a350,0x0,0x0,
  0x20230,0x2a3a0,0x0,0x0,0x20220,0x2a3b0,0x0,0x0,0x20210,0x2a380,0x0,0x0,0x20200,0x2a390,0x0,0x0,
  0x20270,0x2a3e0,0x0,0x0,0x20260,0x2a3f0,0x0,0x0,0x20250,0x2a3c0,0x0,0x0,0x20240,0x2a3d0,0x0,0x0,
  0x2a000,0x2a420,0x0,0x0,0x2a010,0x2a430,0x0,0x0,0x2a020,0x2a400,0x0,0x0,0x2a030,0x2a410,0x0,0x0,
  0x2a040,0x2a460,0x0,0x0,0x2a050,0x2a470,0x0,0x0,0x2a060,0x2a440,0x0,0x0,0x2a070,0x2a450,0x0,0x0,
  0x2a080,0x2a4b0,0x0,0x0,0x2a090,0x2a4a0,0x0,0x0,0x2a0a0,0x2a490,0x0,0x0,0x2a0b0,0x2a480,0x0,0x0,
  0x2a0c0,0x2a4f0,0x0,0x0,0x2a0d0,0x2a4e0,0x0,0x0,0x2a0e0,0x2a4d0,0x0,0x0,0x2a0f0,0x2a4c0,0x0,0x0,
  0x2a100,0x2a530,0x0,0x0,0x2a110,0x2a520,0x0,0x0,0x2a120,0x2a510,0x0,0x0,0x2a130,0x2a500,0x0,0x0,
  0x2a140,0x2a570,0x0,0x0,0x2a150,0x2a560,0x0,0x0,0x2a160,0x2a550,0x0,0x0,0x2a170,0x2a540,0x0,0x0,
  0x2a190,0x2a5b0,0x0,0x0,0x2a180,0x2a5a0,0x0,0x0,0x2a1b0,0x2a590,0x0,0x0,0x2a1a0,0x2a580,0x0,0x0,
  0x2a1d0,0x2a5f0,0x0,0x0,0x2a1c0,0x2a5e0,0x0,0x0,0x2a1f0,0x2a5d0,0x0,0x0,0x2a1e0,0x2a5c0,0x0,0x0,
  0x3f120,0x3f510,0x0,0x0,0x3f130,0x3f500,0x0,0x0,0x3f100,0x3f530,0x0,0x0,0x3f110,0x3f520,0x0,0x0,
  0x3f160,0x3f550,0x0,0x0,0x3f170,0x3f540,0x0,0x0,0x3f140,0x3f570,0x0,0x0,0x3f150,0x3f560,0x0,0x0,
  0x3f1b0,0x3f590,0x0,0x0,0x3f1a0,0x3f580,0x0,0x0,0x3f190,0x3f5b0,0x0,0x0,0x3f180,0x3f5a0,0x0,0x0,
  0x3f1f0,0x3f5d0,0x0,0x0,0x3f1e0,0x3f5c0,0x0,0x0,0x3f1d0,0x3f5f0,0x0,0x0,0x3f1c0,0x3f5e0,0x0,0x0,
  0x3f230,0x3f620,0x0,0x0,0x3f220,0x3f630,0x0,0x0,0x3f210,0x3f600,0x0,0x0,0x3f200,0x3f610,0x0,0x0,
  0x3f270,0x3f660,0x0,0x0,0x3f260,0x3f670,0x0,0x0,0x3f250,0x3f640,0x0,0x0,0x3f240,0x3f650,0x0,0x0,
  0x3f2b0,0x3f6a0,0x0,0x0,0x3f2a0,0x3f6b0,0x0,0x0,0x3f290,0x3f680,0x0,0x0,0x3f280,0x3f690,0x0,0x0,
  0x3f2f0,0x3f6e0,0x0,0x0,0x3f2e0,0x3f6f0,0x0,0x0,0x3f2d0,0x3f6c0,0x0,0x0,0x3f2c0,0x3f6d0,0x0,0x0,
  0x3f300,0x3f720,0x0,0x0,0x3f310,0x3f730,0x0,0x0,0x3f320,0x3f700,0x0,0x0,0x3f330,0x3f710,0x0,0x0,
  0x3f340,0x3f760,0x0,0x0,0x3f350,0x3f770,0x0,0x0,0x3f360,0x3f740,0x0,0x0,0x3f370,0x3f750,0x0,0x0,
  0x3f380,0x3f7b0,0x0,0x0,0x3f390,0x3f7a0,0x0,0x0,0x3f3a0,0x3f790,0x0,0x0,0x3f3b0,0x3f780,0x0,0x0,
  0x3f3c0,0x3f7f0,0x0,0x0,0x3f3d0,0x3f7e0,0x0,0x0,0x3f3e0,0x3f7d0,0x0,0x0,0x3f3f0,0x3f7c0,0x0,0x0,
  0x3f400,0x3f830,0x0,0x0,0x3f410,0x3f820,0x0,0x0,0x3f420,0x3f810,0x0,0x0,0x3f430,0x3f800,0x0,0x0,
  0x3f440,0x3f870,0x0,0x0,0x3f450,0x3f860,0x0,0x0,0x3f460,0x3f850,0x0,0x0,0x3f470,0x3f840,0x0,0x0,
  0x3f490,0x3f8b0,0x0,0x0,0x3f480,0x3f8a0,0x0,0x0,0x3f4b0,0x3f890,0x0,0x0,0x3f4a0,0x3f880,0x0,0x0,
  0x3f4d0,0x3f8f0,0x0,0x0,0x3f4c0,0x3f8e0,0x0,0x0,0x3f4f0,0x3f8d0,0x0,0x0,0x3f4e0,0x3f8c0,0x0,0x0,
  0x54420,0x54810,0x0,0x0,0x54430,0x54800,0x0,0x0,0x54400,0x54830,0x0,0x0,0x54410,0x54820,0x0,0x0,
  0x54460,0x54850,0x0,0x0,0x54470,0x54840,0x0,0x0,0x54440,0x54870,0x0,0x0,0x54450,0x54860,0x0,0x0,
  0x544b0,0x54890,0x0,0x0,0x544a0,0x54880,0x0,0x0,0x54490,0x548b0,0x0,0x0,0x54480,0x548a0,0x0,0x0,
  0x544f0,0x548d0,0x0,0x0,0x544e0,0x548c0,0x0,0x0,0x544d0,0x548f0,0x0,0x0,0x544c0,0x548e0,0x0,0x0,
  0x54530,0x54920,0x0,0x0,0x54520,0x54930,0x0,0x0,0x54510,0x54900,0x0,0x0,0x54500,0x54910,0x0,0x0,
  0x54570,0x54960,0x0,0x0,0x54560,0x54970,0x0,0x0,0x54550,0x54940,0x0,0x0,0x54540,0x54950,0x0,0x0,
  0x545b0,0x549a0,0x0,0x0,0x545a0,0x549b0,0x0,0x0,0x54590,0x54980,0x0,0x0,0x54580,0x54990,0x0,0x0,
  0x545f0,0x549e0,0x0,0x0,0x545e0,0x549f0,0x0,0x0,0x545d0,0x549c0,0x0,0x0,0x545c0,0x549d0,0x0,0x0,
  0x54600,0x54a20,0x0,0x0,0x54610,0x54a30,0x0,0x0,0x54620,0x54a00,0x0,0x0,0x54630,0x54a10,0x0,0x0,
  0x54640,0x54a60,0x0,0x0,0x54650,0x54a70,0x0,0x0,0x54660,0x54a40,0x0,0x0,0x54670,0x54a50,0x0,0x0,
  0x54680,0x5e830,0x0,0x0,0x54690,0x5e820,0x0,0x0,0x546a0,0x5e810,0x0,0x0,0x546b0,0x5e800,0x0,0x0,
  0x546c0,0x5e870,0x0,0x0,0x546d0,0x5e860,0x0,0x0,0x546e0,0x5e850,0x0,0x0,0x546f0,0x5e840,0x0,0x0,
  0x54700,0x5e8b0,0x0,0x0,0x54710,0x5e8a0,0x0,0x0,0x54720,0x5e890,0x0,0x0,0x54730,0x5e880,0x0,0x0,
  0x54740,0x5e8f0,0x0,0x0,0x54750,0x5e8e0,0x0,0x0,0x54760,0x5e8d0,0x0,0x0,0x54770,0x5e8c0,0x0,0x0,
  0x54790,0x5e930,0x0,0x0,0x54780,0x5e920,0x0,0x0,0x547b0,0x5e910,0x0,0x0,0x547a0,0x5e900,0x0,0x0,
  0x547d0,0x5e970,0x0,0x0,0x547c0,0x5e960,0x0,0x0,0x547f0,0x5e950,0x0,0x0,0x547e0,0x5e940,0x0,0x0,
  0x69720,0x73890,0x0,0x0,0x69730,0x73880,0x0,0x0,0x69700,0x738b0,0x0,0x0,0x69710,0x738a0,0x0,0x0,
  0x69760,0x738d0,0x0,0x0,0x69770,0x738c0,0x0,0x0,0x69740,0x738f0,0x0,0x0,0x69750,0x738e0,0x0,0x0,
  0x697b0,0x73910,0x0,0x0,0x697a0,0x73900,0x0,0x0,0x69790,0x73930,0x0,0x0,0x69780,0x73920,0x0,0x0,
  0x697f0,0x73950,0x0,0x0,0x697e0,0x73940,0x0,0x0,0x697d0,0x73970,0x0,0x0,0x697c0,0x73960,0x0,0x0,
  0x69830,0x739a0,0x0,0x0,0x69820,0x739b0,0x0,0x0,0x69810,0x73980,0x0,0x0,0x69800,0x73990,0x0,0x0,
  0x69870,0x739e0,0x0,0x0,0x69860,0x739f0,0x0,0x0,0x69850,0x739c0,0x0,0x0,0x69840,0x739d0,0x0,0x0,
  0x698b0,0x73a20,0x0,0x0,0x698a0,0x73a30,0x0,0x0,0x69890,0x73a00,0x0,0x0,0x69880,0x73a10,0x0,0x0,
  0x698f0,0x73a60,0x0,0x0,0x698e0,0x73a70,0x0,0x0,0x698d0,0x73a40,0x0,0x0,0x698c0,0x73a50,0x0,0x0,
  0x69900,0x73aa0,0x0,0x0,0x69910,0x73ab0,0x0,0x0,0x69920,0x73a80,0x0,0x0,0x69930,0x73a90,0x0,0x0,
  0x69940,0x73ae0,0x0,0x0,0x69950,0x73af0,0x0,0x0,0x69960,0x73ac0,0x0,0x0,0x69970,0x73ad0,0x0,0x0,
  0x69980,0x73b30,0x0,0x0,0x69990,0x73b20,0x0,0x0,0x699a0,0x73b10,0x0,0x0,0x699b0,0x73b00,0x0,0x0,
  0x699c0,0x73b70,0x0,0x0,0x699d0,0x73b60,0x0,0x0,0x699e0,0x73b50,0x0,0x0,0x699f0,0x73b40,0x0,0x0,
  0x69a00,0x73bb0,0x0,0x0,0x69a10,0x73ba0,0x0,0x0,0x69a20,0x73b90,0x0,0x0,0x69a30,0x73b80,0x0,0x0,
  0x69a40,0x73bf0,0x0,0x0,0x69a50,0x73be0,0x0,0x0,0x69a60,0x73bd0,0x0,0x0,0x69a70,0x73bc0,0x0,0x0,
  0x73810,0x73c30,0x0,0x0,0x73800,0x73c20,0x0,0x0,0x73830,0x73c10,0x0,0x0,0x73820,0x73c00,0x0,0x0,
  0x73850,0x73c70,0x0,0x0,0x73840,0x73c60,0x0,0x0,0x73870,0x73c50,0x0,0x0,0x73860,0x73c40,0x0,0x0,
  0x7ea20,0x88b90,0x0,0x0,0x7ea30,0x88b80,0x0,0x0,0x7ea00,0x88bb0,0x0,0x0,0x7ea10,0x88ba0,0x0,0x0,
  0x7ea60,0x88bd0,0x0,0x0,0x7ea70,0x88bc0,0x0,0x0,0x7ea40,0x88bf0,0x0,0x0,0x7ea50,0x88be0,0x0,0x0,
  0x88830,0x88c10,0x0,0x0,0x88820,0x88c00,0x0,0x0,0x88810,0x88c30,0x0,0x0,0x88800,0x88c20,0x0,0x0,
  0x88870,0x88c50,0x0,0x0,0x88860,0x88c40,0x0,0x0,0x88850,0x88c70,0x0,0x0,0x88840,0x88c60,0x0,0x0,
  0x888b0,0x88ca0,0x0,0x0,0x888a0,0x88cb0,0x0,0x0,0x88890,0x88c80,0x0,0x0,0x88880,0x88c90,0x0,0x0,
  0x888f0,0x88ce0,0x0,0x0,0x888e0,0x88cf0,0x0,0x0,0x888d0,0x88cc0,0x0,0x0,0x888c0,0x88cd0,0x0,0x0,
  0x88930,0x88d20,0x0,0x0,0x88920,0x88d30,0x0,0x0,0x88910,0x88d00,0x0,0x0,0x88900,0x88d10,0x0,0x0,
  0x88970,0x88d60,0x0,0x0,0x88960,0x88d70,0x0,0x0,0x88950,0x88d40,0x0,0x0,0x88940,0x88d50,0x0,0x0,
  0x88980,0x88da0,0x0,0x0,0x88990,0x88db0,0x0,0x0,0x889a0,0x88d80,0x0,0x0,0x889b0,0x88d90,0x0,0x0,
  0x889c0,0x88de0,0x0,0x0,0x889d0,0x88df0,0x0,0x0,0x889e0,0x88dc0,0x0,0x0,0x889f0,0x88dd0,0x0,0x0,
  0x88a00,0x88e30,0x0,0x0,0x88a10,0x88e20,0x0,0x0,0x88a20,0x88e10,0x0,0x0,0x88a30,0x88e00,0x0,0x0,
  0x88a40,0x88e70,0x0,0x0,0x88a50,0x88e60,0x0,0x0,0x88a60,0x88e50,0x0,0x0,0x88a70,0x88e40,0x0,0x0,
  0x88a80,0x88eb0,0x0,0x0,0x88a90,0x88ea0,0x0,0x0,0x88aa0,0x88e90,0x0,0x0,0x88ab0,0x88e80,0x0,0x0,
  0x88ac0,0x88ef0,0x0,0x0,0x88ad0,0x88ee0,0x0,0x0,0x88ae0,0x88ed0,0x0,0x0,0x88af0,0x88ec0,0x0,0x0,
  0x88b10,0x88f30,0x0,0x0,0x88b00,0x88f20,0x0,0x0,0x88b30,0x88f10,0x0,0x0,0x88b20,0x88f00,0x0,0x0,
  0x88b50,0x88f70,0x0,0x0,0x88b40,0x88f60,0x0,0x0,0x88b70,0x88f50,0x0,0x0,0x88b60,0x88f40,0x0,0x0,
  0x0,0xa980,0x0,0x20,0x10,0xa990,0x40,0x60,0x20,0xa9a0,0x80,0xa0,0x30,0xa9b0,0xc0,0xe0,
  0xc0,0xaa40,0x110,0x130,0xd0,0xaa50,0x150,0x170,0xe0,0xaa60,0x190,0x1b0,0xf0,0xaa70,0x1d0,0x1f0,
  0x180,0xab00,0x220,0x200,0x190,0xab10,0x260,0x240,0x1a0,0xab20,0x2a0,0x280,0x1b0,0xab30,0x2e0,0x2c0,
  0x240,0xabc0,0x330,0x310,0x250,0xabd0,0x370,0x350,0x260,0xabe0,0x3b0,0x390,0x270,0xabf0,0x3f0,0x3d0,
  0x310,0xac90,0x400,0x420,0x300,0xac80,0x440,0x460,0x330,0xacb0,0x480,0x4a0,0x320,0xaca0,0x4c0,0x4e0,
  0x3d0,0xad50,0x510,0x530,0x3c0,0xad40,0x550,0x570,0x3f0,0xad70,0x590,0x5b0,0x3e0,0xad60,0x5d0,0x5f0,
  0x490,0xae10,0x620,0x600,0x480,0xae00,0x660,0x640,0x4b0,0xae30,0x6a0,0x680,0x4a0,0xae20,0x6e0,0x6c0,
  0x550,0xaed0,0x730,0x710,0x540,0xaec0,0x770,0x750,0x570,0xaef0,0x7b0,0x790,0x560,0xaee0,0x7f0,0x7d0,
  0x620,0xafa0,0x10,0x30,0x630,0xafb0,0x50,0x70,0x600,0xaf80,0x90,0xb0,0x610,0xaf90,0xd0,0xf0,
  0x6e0,0xb060,0x100,0x120,0x6f0,0xb070,0x140,0x160,0x6c0,0xb040,0x180,0x1a0,0x6d0,0xb050,0x1c0,0x1e0,
  0x7a0,0xb120,0x230,0x210,0x7b0,0xb130,0x270,0x250,0x780,0xb100,0x2b0,0x290,0x790,0xb110,0x2f0,0x2d0,
  0x860,0xb1e0,0x320,0x300,0x870,0xb1f0,0x360,0x340,0x840,0xb1c0,0x3a0,0x380,0x850,0xb1d0,0x3e0,0x3c0,
  0x930,0x15030,0x410,0x430,0x920,0x15020,0x450,0x470,0x910,0x15010,0x490,0x4b0,0x900,0x15000,0x4d0,0x4f0,
  0x9f0,0x150f0,0x500,0x520,0x9e0,0x150e0,0x540,0x560,0x9d0,0x150d0,0x580,0x5a0,0x9c0,0x150c0,0x5c0,0x5e0,
  0xa830,0x151b0,0x630,0x610,0xa820,0x151a0,0x670,0x650,0xa810,0x15190,0x6b0,0x690,0xa800,0x15180,0x6f0,0x6d0,
  0xa8f0,0x15270,0x720,0x700,0xa8e0,0x15260,0x760,0x740,0xa8d0,0x15250,0x7a0,0x780,0xa8c0,0x15240,0x7e0,0x7c0,
  0x15300,0x1fc80,0x800,0x820,0x15310,0x1fc90,0x840,0x860,0x15320,0x1fca0,0x880,0x8a0,0x15330,0x1fcb0,0x8c0,0x8e0,
  0x153c0,0x1fd40,0x910,0x930,0x153d0,0x1fd50,0x950,0x970,0x153e0,0x1fd60,0x990,0x9b0,0x153f0,0x1fd70,0x9d0,0x9f0,
  0x15480,0x1fe00,0xa20,0xa00,0x15490,0x1fe10,0xa60,0xa40,0x154a0,0x1fe20,0xaa0,0xa80,0x154b0,0x1fe30,0xae0,0xac0,
  0x15540,0x1fec0,0xb30,0xb10,0x15550,0x1fed0,0xb70,0xb50,0x15560,0x1fee0,0xbb0,0xb90,0x15570,0x1fef0,0xbf0,0xbd0,
  0x15610,0x1ff90,0xc00,0xc20,0x15600,0x1ff80,0xc40,0xc60,0x15630,0x1ffb0,0xc80,0xca0,0x15620,0x1ffa0,0xcc0,0xce0,
  0x156d0,0x20050,0xd10,0xd30,0x156c0,0x20040,0xd50,0xd70,0x156f0,0x20070,0xd90,0xdb0,0x156e0,0x20060,0xdd0,0xdf0,
  0x15790,0x20110,0xe20,0xe00,0x15780,0x20100,0xe60,0xe40,0x157b0,0x20130,0xea0,0xe80,0x157a0,0x20120,0xee0,0xec0,
  0x15850,0x201d0,0xf30,0xf10,0x15840,0x201c0,0xf70,0xf50,0x15870,0x201f0,0xfb0,0xf90,0x15860,0x201e0,0xff0,0xfd0,
  0x15920,0x2a020,0x810,0x830,0x15930,0x2a030,0x850,0x870,0x15900,0x2a000,0x890,0x8b0,0x15910,0x2a010,0x8d0,0x8f0,
  0x159e0,0x2a0e0,0x900,0x920,0x159f0,0x2a0f0,0x940,0x960,0x159c0,0x2a0c0,0x980,0x9a0,0x159d0,0x2a0d0,0x9c0,0x9e0,
  0x1f820,0x2a1a0,0xa30,0xa10,0x1f830,0x2a1b0,0xa70,0xa50,0x1f800,0x2a180,0xab0,0xa90,0x1f810,0x2a190,0xaf0,0xad0,
  0x1f8e0,0x2a260,0xb20,0xb00,0x1f8f0,0x2a270,0xb60,0xb40,0x1f8c0,0x2a240,0xba0,0xb80,0x1f8d0,0x2a250,0xbe0,0xbc0,
  0x1f9b0,0x2a330,0xc10,0xc30,0x1f9a0,0x2a320,0xc50,0xc70,0x1f990,0x2a310,0xc90,0xcb0,0x1f980,0x2a300,0xcd0,0xcf0,
  0x1fa70,0x2a3f0,0xd00,0xd20,0x1fa60,0x2a3e0,0xd40,0xd60,0x1fa50,0x2a3d0,0xd80,0xda0,0x1fa40,0x2a3c0,0xdc0,0xde0,
  0x1fb30,0x2a4b0,0xe30,0xe10,0x1fb20,0x2a4a0,0xe70,0xe50,0x1fb10,0x2a490,0xeb0,0xe90,0x1fb00,0x2a480,0xef0,0xed0,
  0x1fbf0,0x2a570,0xf20,0xf00,0x1fbe0,0x2a560,0xf60,0xf40,0x1fbd0,0x2a550,0xfa0,0xf80,0x1fbc0,0x2a540,0xfe0,0xfc0,
  0x2a600,0x34f80,0x1000,0x1020,0x2a610,0x34f90,0x1040,0x1060,0x2a620,0x34fa0,0x1080,0x10a0,0x2a630,0x34fb0,0x10c0,0x10e0,
  0x2a6c0,0x35040,0x1110,0x1130,0x2a6d0,0x35050,0x1150,0x1170,0x2a6e0,0x35060,0x1190,0x11b0,0x2a6f0,0x35070,0x11d0,0x11f0,
  0x2a780,0x35100,0x1220,0x1200,0x2a790,0x35110,0x1260,0x1240,0x2a7a0,0x35120,0x12a0,0x1280,0x2a7b0,0x35130,0x12e0,0x12c0,
  0x2a840,0x351c0,0x1330,0x1310,0x2a850,0x351d0,0x1370,0x1350,0x2a860,0x351e0,0x13b0,0x1390,0x2a870,0x351f0,0x13f0,0x13d0,
  0x2a910,0x3f010,0x1400,0x1420,0x2a900,0x3f000,0x1440,0x1460,0x2a930,0x3f030,0x1480,0x14a0,0x2a920,0x3f020,0x14c0,0x14e0,
  0x2a9d0,0x3f0d0,0x1510,0x1530,0x2a9c0,0x3f0c0,0x1550,0x1570,0x2a9f0,0x3f0f0,0x1590,0x15b0,0x2a9e0,0x3f0e0,0x15d0,0x15f0,
  0x34810,0x3f190,0x1620,0x1600,0x34800,0x3f180,0x1660,0x1640,0x34830,0x3f1b0,0x16a0,0x1680,0x34820,0x3f1a0,0x16e0,0x16c0,
  0x348d0,0x3f250,0x1730,0x1710,0x348c0,0x3f240,0x1770,0x1750,0x348f0,0x3f270,0x17b0,0x1790,0x348e0,0x3f260,0x17f0,0x17d0,
  0x349a0,0x3f320,0x1010,0x1030,0x349b0,0x3f330,0x1050,0x1070,0x34980,0x3f300,0x1090,0x10b0,0x34990,0x3f310,0x10d0,0x10f0,
  0x34a60,0x3f3e0,0x1100,0x1120,0x34a70,0x3f3f0,0x1140,0x1160,0x34a40,0x3f3c0,0x1180,0x11a0,0x34a50,0x3f3d0,0x11c0,0x11e0,
  0x34b20,0x3f4a0,0x1230,0x1210,0x34b30,0x3f4b0,0x1270,0x1250,0x34b00,0x3f480,0x12b0,0x1290,0x34b10,0x3f490,0x12f0,0x12d0,
  0x34be0,0x3f560,0x1320,0x1300,0x34bf0,0x3f570,0x1360,0x1340,0x34bc0,0x3f540,0x13a0,0x1380,0x34bd0,0x3f550,0x13e0,0x13c0,
  0x34cb0,0x3f630,0x1410,0x1430,0x34ca0,0x3f620,0x1450,0x1470,0x34c90,0x3f610,0x1490,0x14b0,0x34c80,0x3f600,0x14d0,0x14f0,
  0x34d70,0x3f6f0,0x1500,0x1520,0x34d60,0x3f6e0,0x1540,0x1560,0x34d50,0x3f6d0,0x1580,0x15a0,0x34d40,0x3f6c0,0x15c0,0x15e0,
  0x34e30,0x3f7b0,0x1630,0x1610,0x34e20,0x3f7a0,0x1670,0x1650,0x34e10,0x3f790,0x16b0,0x1690,0x34e00,0x3f780,0x16f0,0x16d0,
  0x34ef0,0x3f870,0x1720,0x1700,0x34ee0,0x3f860,0x1760,0x1740,0x34ed0,0x3f850,0x17a0,0x1780,0x34ec0,0x3f840,0x17e0,0x17c0,
  0x3f900,0x54000,0x1800,0x1820,0x3f910,0x54010,0x1840,0x1860,0x3f920,0x54020,0x1880,0x18a0,0x3f930,0x54030,0x18c0,0x18e0,
  0x3f9c0,0x540c0,0x1910,0x1930,0x3f9d0,0x540d0,0x1950,0x1970,0x3f9e0,0x540e0,0x1990,0x19b0,0x3f9f0,0x540f0,0x19d0,0x19f0,
  0x49800,0x54180,0x1a20,0x1a00,0x49810,0x54190,0x1a60,0x1a40,0x49820,0x541a0,0x1aa0,0x1a80,0x49830,0x541b0,0x1ae0,0x1ac0,
  0x498c0,0x54240,0x1b30,0x1b10,0x498d0,0x54250,0x1b70,0x1b50,0x498e0,0x54260,0x1bb0,0x1b90,0x498f0,0x54270,0x1bf0,0x1bd0,
  0x49990,0x54310,0x1c00,0x1c20,0x49980,0x54300,0x1c40,0x1c60,0x499b0,0x54330,0x1c80,0x1ca0,0x499a0,0x54320,0x1cc0,0x1ce0,
  0x49a50,0x543d0,0x1d10,0x1d30,0x49a40,0x543c0,0x1d50,0x1d70,0x49a70,0x543f0,0x1d90,0x1db0,0x49a60,0x543e0,0x1dd0,0x1df0,
  0x49b10,0x54490,0x1e20,0x1e00,0x49b00,0x54480,0x1e60,0x1e40,0x49b30,0x544b0,0x1ea0,0x1e80,0x49b20,0x544a0,0x1ee0,0x1ec0,
  0x49bd0,0x54550,0x1f30,0x1f10,0x49bc0,0x54540,0x1f70,0x1f50,0x49bf0,0x54570,0x1fb0,0x1f90,0x49be0,0x54560,0x1ff0,0x1fd0,
  0x49ca0,0x54620,0x1810,0x1830,0x49cb0,0x54630,0x1850,0x1870,0x49c80,0x54600,0x1890,0x18b0,0x49c90,0x54610,0x18d0,0x18f0,
  0x49d60,0x546e0,0x1900,0x1920,0x49d70,0x546f0,0x1940,0x1960,0x49d40,0x546c0,0x1980,0x19a0,0x49d50,0x546d0,0x19c0,0x19e0,
  0x49e20,0x547a0,0x1a30,0x1a10,0x49e30,0x547b0,0x1a70,0x1a50,0x49e00,0x54780,0x1ab0,0x1a90,0x49e10,0x54790,0x1af0,0x1ad0,
  0x49ee0,0x54860,0x1b20,0x1b00,0x49ef0,0x54870,0x1b60,0x1b40,0x49ec0,0x54840,0x1ba0,0x1b80,0x49ed0,0x54850,0x1be0,0x1bc0,
  0x49fb0,0x54930,0x1c10,0x1c30,0x49fa0,0x54920,0x1c50,0x1c70,0x49f90,0x54910,0x1c90,0x1cb0,0x49f80,0x54900,0x1cd0,0x1cf0,
  0x4a070,0x549f0,0x1d00,0x1d20,0x4a060,0x549e0,0x1d40,0x1d60,0x4a050,0x549d0,0x1d80,0x1da0,0x4a040,0x549c0,0x1dc0,0x1de0,
  0x4a130,0x5e830,0x1e30,0x1e10,0x4a120,0x5e820,0x1e70,0x1e50,0x4a110,0x5e810,0x1eb0,0x1e90,0x4a100,0x5e800,0x1ef0,0x1ed0,
  0x4a1f0,0x5e8f0,0x1f20,0x1f00,0x4a1e0,0x5e8e0,0x1f60,0x1f40,0x4a1d0,0x5e8d0,0x1fa0,0x1f80,0x4a1c0,0x5e8c0,0x1fe0,0x1fc0,
  0x5e980,0x69300,0x2000,0x2020,0x5e990,0x69310,0x2040,0x2060,0x5e9a0,0x69320,0x2080,0x20a0,0x5e9b0,0x69330,0x20c0,0x20e0,
  0x5ea40,0x693c0,0x2110,0x2130,0x5ea50,0x693d0,0x2150,0x2170,0x5ea60,0x693e0,0x2190,0x21b0,0x5ea70,0x693f0,0x21d0,0x21f0,
  0x5eb00,0x69480,0x2220,0x2200,0x5eb10,0x69490,0x2260,0x2240,0x5eb20,0x694a0,0x22a0,0x2280,0x5eb30,0x694b0,0x22e0,0x22c0,
  0x5ebc0,0x69540,0x2330,0x2310,0x5ebd0,0x69550,0x2370,0x2350,0x5ebe0,0x69560,0x23b0,0x2390,0x5ebf0,0x69570,0x23f0,0x23d0,
  0x5ec90,0x69610,0x2400,0x2420,0x5ec80,0x69600,0x2440,0x2460,0x5ecb0,0x69630,0x2480,0x24a0,0x5eca0,0x69620,0x24c0,0x24e0,
  0x5ed50,0x696d0,0x2510,0x2530,0x5ed40,0x696c0,0x2550,0x2570,0x5ed70,0x696f0,0x2590,0x25b0,0x5ed60,0x696e0,0x25d0,0x25f0,
  0x5ee10,0x69790,0x2620,0x2600,0x5ee00,0x69780,0x2660,0x2640,0x5ee30,0x697b0,0x26a0,0x2680,0x5ee20,0x697a0,0x26e0,0x26c0,
  0x5eed0,0x69850,0x2730,0x2710,0x5eec0,0x69840,0x2770,0x2750,0x5eef0,0x69870,0x27b0,0x2790,0x5eee0,0x69860,0x27f0,0x27d0,
  0x5efa0,0x69920,0x2010,0x2030,0x5efb0,0x69930,0x2050,0x2070,0x5ef80,0x69900,0x2090,0x20b0,0x5ef90,0x69910,0x20d0,0x20f0,
  0x5f060,0x699e0,0x2100,0x2120,0x5f070,0x699f0,0x2140,0x2160,0x5f040,0x699c0,0x2180,0x21a0,0x5f050,0x699d0,0x21c0,0x21e0,
  0x5f120,0x73820,0x2230,0x2210,0x5f130,0x73830,0x2270,0x2250,0x5f100,0x73800,0x22b0,0x2290,0x5f110,0x73810,0x22f0,0x22d0,
  0x5f1e0,0x738e0,0x2320,0x2300,0x5f1f0,0x738f0,0x2360,0x2340,0x5f1c0,0x738c0,0x23a0,0x2380,0x5f1d0,0x738d0,0x23e0,0x23c0,
  0x69030,0x739b0,0x2410,0x2430,0x69020,0x739a0,0x2450,0x2470,0x69010,0x73990,0x2490,0x24b0,0x69000,0x73980,0x24d0,0x24f0,
  0x690f0,0x73a70,0x2500,0x2520,0x690e0,0x73a60,0x2540,0x2560,0x690d0,0x73a50,0x2580,0x25a0,0x690c0,0x73a40,0x25c0,0x25e0,
  0x691b0,0x73b30,0x2630,0x2610,0x691a0,0x73b20,0x2670,0x2650,0x69190,0x73b10,0x26b0,0x2690,0x69180,0x73b00,0x26f0,0x26d0,
  0x69270,0x73bf0,0x2720,0x2700,0x69260,0x73be0,0x2760,0x2740,0x69250,0x73bd0,0x27a0,0x2780,0x69240,0x73bc0,0x27e0,0x27c0,
  0x73c80,0x7e600,0x2800,0x2820,0x73c90,0x7e610,0x2840,0x2860,0x73ca0,0x7e620,0x2880,0x28a0,0x73cb0,0x7e630,0x28c0,0x28e0,
  0x73d40,0x7e6c0,0x2910,0x2930,0x73d50,0x7e6d0,0x2950,0x2970,0x73d60,0x7e6e0,0x2990,0x29b0,0x73d70,0x7e6f0,0x29d0,0x29f0,
  0x73e00,0x7e780,0x2a20,0x2a00,0x73e10,0x7e790,0x2a60,0x2a40,0x73e20,0x7e7a0,0x2aa0,0x2a80,0x73e30,0x7e7b0,0x2ae0,0x2ac0,
  0x73ec0,0x7e840,0x2b30,0x2b10,0x73ed0,0x7e850,0x2b70,0x2b50,0x73ee0,0x7e860,0x2bb0,0x2b90,0x73ef0,0x7e870,0x2bf0,0x2bd0,
  0x73f90,0x7e910,0x2c00,0x2c20,0x73f80,0x7e900,0x2c40,0x2c60,0x73fb0,0x7e930,0x2c80,0x2ca0,0x73fa0,0x7e920,0x2cc0,0x2ce0,
  0x74050,0x7e9d0,0x2d10,0x2d30,0x74040,0x7e9c0,0x2d50,0x2d70,0x74070,0x7e9f0,0x2d90,0x2db0,0x74060,0x7e9e0,0x2dd0,0x2df0,
  0x74110,0x88810,0x2e20,0x2e00,0x74100,0x88800,0x2e60,0x2e40,0x74130,0x88830,0x2ea0,0x2e80,0x74120,0x88820,0x2ee0,0x2ec0,
  0x741d0,0x888d0,0x2f30,0x2f10,0x741c0,0x888c0,0x2f70,0x2f50,0x741f0,0x888f0,0x2fb0,0x2f90,0x741e0,0x888e0,0x2ff0,0x2fd0,
  0x7e020,0x889a0,0x2810,0x2830,0x7e030,0x889b0,0x2850,0x2870,0x7e000,0x88980,0x2890,0x28b0,0x7e010,0x88990,0x28d0,0x28f0,
  0x7e0e0,0x88a60,0x2900,0x2920,0x7e0f0,0x88a70,0x2940,0x2960,0x7e0c0,0x88a40,0x2980,0x29a0,0x7e0d0,0x88a50,0x29c0,0x29e0,
  0x7e1a0,0x88b20,0x2a30,0x2a10,0x7e1b0,0x88b30,0x2a70,0x2a50,0x7e180,0x88b00,0x2ab0,0x2a90,0x7e190,0x88b10,0x2af0,0x2ad0,
  0x7e260,0x88be0,0x2b20,0x2b00,0x7e270,0x88bf0,0x2b60,0x2b40,0x7e240,0x88bc0,0x2ba0,0x2b80,0x7e250,0x88bd0,0x2be0,0x2bc0,
  0x7e330,0x88cb0,0x2c10,0x2c30,0x7e320,0x88ca0,0x2c50,0x2c70,0x7e310,0x88c90,0x2c90,0x2cb0,0x7e300,0x88c80,0x2cd0,0x2cf0,
  0x7e3f0,0x88d70,0x2d00,0x2d20,0x7e3e0,0x88d60,0x2d40,0x2d60,0x7e3d0,0x88d50,0x2d80,0x2da0,0x7e3c0,0x88d40,0x2dc0,0x2de0,
  0x7e4b0,0x88e30,0x2e30,0x2e10,0x7e4a0,0x88e20,0x2e70,0x2e50,0x7e490,0x88e10,0x2eb0,0x2e90,0x7e480,0x88e00,0x2ef0,0x2ed0,
  0x7e570,0x88ef0,0x2f20,0x2f00,0x7e560,0x88ee0,0x2f60,0x2f40,0x7e550,0x88ed0,0x2fa0,0x2f80,0x7e540,0x88ec0,0x2fe0,0x2fc0,
  0x0,0x1400,0x80,0x25000,0x3,0x6200,0xffff9e00,0x1800,0xffff8800,0x0,0x0,0x16000,0x4000,0x1c000,0x1c000,0x0,
  0xc30,0xc40,0x4000,0x5e20,0x4000,0x4010,0xb,0x22000,0x22200,0x1c000,0x22000,0x23800,0x1c000,0x1c000,0x22000,0x0,
  0x4800,0x1400,0x80,0x25c00,0x3,0x6200,0xffff9e00,0x1800,0xffff8800,0x0,0x0,0x17800,0x5a00,0x1c000,0x1c000,0x400,
  0xc50,0xc48,0x7c40,0x6d20,0x4020,0x4018,0xc,0x1c200,0x16000,0x16200,0x1d800,0x16000,0x17800,0x1d800,0x22000,0x800,
  0x9000,0x1400,0x80,0x26800,0x3,0x6200,0xffff9e00,0x1800,0xffff8800,0x0,0x0,0x19000,0x7400,0x1c000,0x1c000,0x800,
  0xc60,0xc70,0x9a60,0xb880,0x4030,0x4040,0xd,0x10000,0x10200,0xa000,0x10000,0x11800,0xa000,0x1f000,0x25000,0x1000,
  0xd800,0x1400,0x80,0x27400,0x3,0x6200,0xffff9e00,0x1800,0xffff8800,0x0,0x0,0x1a800,0x8e00,0x1c000,0x1c000,0xc00,
  0xc80,0xc78,0xd6a0,0xc780,0x4050,0x4048,0xe,0xa200,0x4000,0x4200,0xb800,0x4000,0x5800,0x20800,0x25000,0x1800,
  0x0,0x0,0x3000,0x22000,0x1,0x0,0xffffa000,0xffffa000,0xffffa000,0x0,0x20,0x0,0xa800,0x1c000,0x1c000,0x1000,
  0xc90,0xca0,0xf4c0,0x112e0,0x4060,0x4070,0xf,0x22000,0x22200,0x1c000,0x22000,0x23800,0x1c000,0x1c000,0x22000,0x2000,
  0xc00,0x0,0x3000,0x22c00,0x1,0x0,0xffffa000,0xffffa000,0xffffa000,0x1,0x20,0x0,0xa800,0x1c200,0x1d800,0x1000,
  0xcb0,0xca8,0x13100,0x121e0,0x4080,0x4078,0x10,0x1c200,0x16000,0x16200,0x1d800,0x16000,0x17800,0x1d800,0x22000,0x2800,
  0x1800,0x0,0x3000,0x23800,0x1,0x0,0xffffa000,0xffffa000,0xffffa000,0x0,0x20,0x1800,0xc200,0x1c000,0x1c000,0x1400,
  0xcc0,0xcd0,0x14f20,0x16d40,0x4090,0x40a0,0x2,0x10000,0x10200,0xa000,0x10000,0x11800,0xa000,0x1f000,0x25000,0x3000,
  0x2400,0x0,0x3000,0x24400,0x1,0x0,0xffffa000,0xffffa000,0xffffa000,0x1,0x20,0x1800,0xc200,0x1c200,0x1d800,0x1400,
  0xce0,0xcd8,0x18b60,0x17c40,0x40b0,0x40a8,0x11,0xa200,0x4000,0x4200,0xb800,0x4000,0x5800,0x20800,0x25000,0x3800,
};

__global__ __launch_bounds__(512, 2) void convnext_block_kernel(const void* x, const void* dw, const void* w1,
                                                                const void* w2, void* out, void* ws) {
  __shared__ __attribute__((aligned(16))) char smem[163840];
  asm volatile(
      ".p2align 3\n"
      "s_load_dwordx2 s[36:37], %0, 0x0\n"
      "s_load_dwordx2 s[40:41], %0, 0x8\n"
      "s_load_dwordx4 s[52:55], %0, 0x10\n"
      "s_load_dwordx2 s[48:49], %0, 0x20\n"
      "s_getpc_b64 s[60:61]\n"
      ".Lcnx_pc:\n"
      "s_bfe_u32 s97, %1, 0x20003\n"
      "s_lshl_b32 s97, s97, 7\n"
      "v_cmp_le_u32 vcc, 448, v0\n"
      "s_cbranch_vccz .Lcnx_np\n"
      "v_lshlrev_b32 v12, 9, v0\n"
      "v_and_b32 v12, 0x7e00, v12\n"
      "v_add_u32 v12, s97, v12\n"
      "v_min_u32 v12, .Lcnx_end-.Lcnx_pc-4, v12\n"
      "global_load_dword v5, v12, s[60:61]\n"
      ".Lcnx_np:\n"
      "v_lshrrev_b32 v14, 6, v0\n"
      "v_and_b32 v6, 63, v0\n"
      "s_bfe_u32 s56, %1, 0x20001\n"
      "s_lshr_b32 s59, %1, 6\n"
      "v_readfirstlane_b32 s96, v14\n"
      "v_lshlrev_b32 v6, 4, v6\n"
      "v_bfe_u32 v1, v0, 4, 2\n"
      "s_lshl2_add_u32 s56, s56, s59\n"
      "s_and_b32 s57, %1, 1\n"
      "s_bfe_u32 s59, %1, 0x30003\n"
      "s_lshl3_add_u32 s57, s57, s59\n"
      "v_lshlrev_b32 v9, 6, v1\n"
      "v_sub_u32 v9, v6, v9\n"
      "v_cmp_gt_u32 vcc, 0x100, v0\n"
      "s_lshl_b32 s97, s96, 10\n"
      "s_add_u32 m0, s97, 108928\n"
      "v_add_u32 v7, s97, v6\n"
      "s_cbranch_vccz .Lcnx_l1\n"
      "global_load_lds_dwordx4 v7, %4\n"
      ".Lcnx_l1:\n"
      "v_bfe_u32 v14, v0, 2, 4\n"
      "v_mul_u32_u24 v15, 13, v14\n"
      "v_lshrrev_b32 v15, 6, v15\n"
      "v_mul_u32_u24 v14, 768, v14\n"
      "v_mul_u32_u24 v15, 3776, v15\n"
      "v_and_b32 v16, 48, v6\n"
      "v_sub_u32 v14, v14, v15\n"
      "s_cmp_eq_u32 s57, 0\n"
      "s_cselect_b32 s64, 0xf0000f, 0\n"
      "s_cselect_b32 s65, 0xf00, 0\n"
      "s_cmp_eq_u32 s57, 15\n"
      "s_cselect_b32 s66, 0xf0000, 0\n"
      "s_cselect_b32 s67, 0xf0000f0, 0\n"
      "s_or_b32 s65, s65, 0xf0000000\n"
      "s_or_b32 s67, s67, 0xf0000000\n"
      "s_or_b64 s[68:69], s[64:65], s[66:67]\n"
      "s_mul_i32 s56, s56, 14\n"
      "s_mul_i32 s57, s57, 14\n"
      "s_mul_i32 s97, s57, 192\n"
      "v_add3_u32 v14, v14, v16, s97\n"
      "v_bfrev_b32 v19, 2\n"
      "v_subrev_u32 v15, 576, v14\n"
      "v_cndmask_b32 v4, v14, v19, s[66:67]\n"
      "v_cndmask_b32 v2, v15, v19, s[64:65]\n"
      "v_cndmask_b32 v3, v15, v19, s[68:69]\n"
      "s_mul_i32 s99, s96, 1344\n"
      "s_add_u32 s97, s56, s96\n"
      "s_nop 0\n"
      "s_mul_i32 s98, s97, 43008\n"
      "s_mov_b32 s39, 0x20000\n"
      "s_mov_b32 s43, 0x20000\n"
      "s_mov_b32 s38, 9633792\n"
      "s_movk_i32 s42, 9408\n"
      "s_lshl_b32 s97, s96, 7\n"
      "s_add_u32 s97, s97, 25600\n"
      "s_mov_b32 exec_lo, 0x0fff0fff\n"
      "s_mov_b32 exec_hi, 0x0fff0fff\n"
      "s_waitcnt lgkmcnt(0)\n"
      "s_cbranch_vccz .Lcnx_l2\n"
      "s_add_u32 s59, s99, 5376\n"
      "buffer_load_dwordx4 v[64:67], v9, s[40:43], s99 offen\n"
      "buffer_load_dwordx4 v[72:75], v9, s[40:43], s59 offen\n"
      "s_mov_b32 exec_hi, 0x0fff\n"
      "buffer_load_dwordx4 v[68:71], v9, s[40:43], s99 offen offset:768\n"
      "buffer_load_dwordx4 v[76:79], v9, s[40:43], s59 offen offset:768\n"
      ".Lcnx_l2:\n"
      "s_mov_b64 exec, -1\n"
      "s_load_dwordx16 s[64:79], %4, s97\n"
      "s_add_u32 s97, s97, 64\n"
      "buffer_load_dwordx4 v[208:211], v2, s[36:39], s98 offen\n"
      "buffer_load_dwordx4 v[212:215], v3, s[36:39], s98 offen offset:192\n"
      "buffer_load_dwordx4 v[216:219], v3, s[36:39], s98 offen offset:384\n"
      "buffer_load_dwordx4 v[220:223], v4, s[36:39], s98 offen\n"
      "s_load_dwordx16 s[80:95], %4, s97\n"
      "v_mov_b32 v56, 0\n"
      "v_mov_b32 v57, 0\n"
      "v_mov_b32 v58, 0\n"
      "v_mov_b32 v59, 0\n"
      "s_mul_i32 s97, s96, 1664\n"
      "v_lshl_add_u32 v8, v1, 4, v9\n"
      "v_add_u32_e64 v8, s97, v8\n"
      "v_add_u32 v10, 113800, v8\n"
      "v_and_b32 v14, 255, v0\n"
      "v_min_u32 v14, 159, v14\n"
      "v_and_b32 v80, 15, v0\n"
      "v_and_b32 v0, 1, v1\n"
      "v_and_b32 v81, 2, v1\n"
      "v_lshlrev_b32 v81, 2, v81\n"
      "v_lshl_add_u32 v81, v0, 5, v81\n"
      "v_sub_u32 v80, v81, v80\n"
      "v_mul_i32_i24 v80, 208, v80\n"
      "v_add_u32 v1, 108928, v6\n"
      "s_lshr_b32 s97, s96, 1\n"
      "s_mul_i32 s97, s97, 48\n"
      "s_bitcmp1_b32 s96, 0\n"
      "s_cselect_b32 s59, 32, 0\n"
      "s_add_u32 s97, s97, s59\n"
      "s_addk_i32 s97, 3120\n"
      "v_add_u32 v15, s97, v80\n"
      "s_setprio 3\n"
      "s_cbranch_vccz .Lcnx_d2\n"
      "s_cmp_lt_u32 s96, 3\n"
      "s_mov_b32 exec_lo, 0x0fff0fff\n"
      "s_mov_b32 exec_hi, 0x0fff0fff\n"
      "s_waitcnt vmcnt(4)\n"
      "ds_write_b128 v8, v[64:67] offset:3120\n"
      "ds_write_b128 v8, v[72:75] offset:9776\n"
      "ds_write_b64 v10, v[72:73] offset:6656\n"
      "ds_write_b64 v10, v[74:75] offset:6664\n"
      "s_cbranch_scc1 .Lcnx_d1\n"
      "s_nop 0\n"
      "ds_write_b64 v10, v[64:65]\n"
      "ds_write_b64 v10, v[66:67] offset:8\n"
      "s_mov_b32 exec_hi, 0x0fff\n"
      "ds_write_b64 v10, v[68:69] offset:832\n"
      "ds_write_b64 v10, v[70:71] offset:840\n"
      ".Lcnx_d1:\n"
      "s_mov_b32 exec_hi, 0x0fff\n"
      "ds_write_b128 v8, v[68:71] offset:3952\n"
      "ds_write_b128 v8, v[76:79] offset:10608\n"
      "ds_write_b64 v10, v[76:77] offset:7488\n"
      "ds_write_b64 v10, v[78:79] offset:7496\n"
      "s_mov_b64 exec, -1\n"
      "s_branch .Lcnx_d3\n"
      ".Lcnx_d2:\n"
      "s_cmp_eq_u32 s96, 7\n"
      "s_cbranch_scc1 .Lcnx_d4\n"
      "v_mul_u32_u24_e64 v14, 48, v14\n"
      "v_add_u32 v14, 47272, v14\n"
      "ds_write_b64 v14, v[56:57] offset:0\n"
      "ds_write_b64 v14, v[56:57] offset:7712\n"
      "ds_write_b64 v14, v[56:57] offset:15424\n"
      "ds_write_b64 v14, v[56:57] offset:23136\n"
      "ds_write_b64 v14, v[56:57] offset:30848\n"
      "ds_write_b64 v14, v[56:57] offset:38560\n"
      "ds_write_b64 v14, v[56:57] offset:46272\n"
      "ds_write_b64 v14, v[56:57] offset:53984\n"
      "s_branch .Lcnx_d3\n"
      ".Lcnx_d4:\n"
      "ds_write_b128 v6, v[56:59] offset:1456\n"
      "ds_write_b128 v6, v[56:59] offset:2096\n"
      ".Lcnx_d3:\n"
      "s_waitcnt lgkmcnt(0)\n"
      "s_barrier\n"
      "s_nop 0\n"
      "ds_read_b128 v[16:19], v1 offset:2048\n"
      "ds_read_b128 v[20:23], v1 offset:3072\n"
      "ds_read_b128 v[76:79], v15 offset:0\n"
      "ds_read_b128 v[92:95], v15 offset:208\n"
      "ds_read_b128 v[108:111], v15 offset:416\n"
      "ds_read_b128 v[124:127], v15 offset:624\n"
      "ds_read_b128 v[140:143], v15 offset:832\n"
      "ds_read_b128 v[156:159], v15 offset:1040\n"
      "ds_read_b128 v[172:175], v15 offset:1248\n"
      "ds_read_b128 v[188:191], v15 offset:1456\n"
      "s_add_u32 s97, s56, s86\n"
      "s_sub_u32 s97, s97, 3\n"
      "s_mul_i32 s98, s97, 43008\n"
      "s_cmp_lt_u32 s97, 224\n"
      "s_cselect_b32 s98, s98, 1073741824\n"
      "s_and_b32 s59, s96, 2\n"
      "s_lshl3_add_u32 s59, s59, s96\n"
      "s_add_u32 s97, s56, s59\n"
      "s_sub_u32 s97, s97, 3\n"
      "s_mul_i32 s99, s97, 43008\n"
      "s_cmp_lt_u32 s97, 224\n"
      "s_cselect_b32 s99, s99, 1073741824\n"
      "v_mul_u32_u24 v0, 110680, v0\n"
      "buffer_load_dwordx4 v[224:227], v2, s[36:39], s98 offen\n"
      "s_waitcnt lgkmcnt(4)\n"
      "s_nop 0\n"
      "v_perm_b32 v64, v92, v76, v16\n"
      "v_perm_b32 v80, v92, v76, v20\n"
      "v_perm_b32 v96, v93, v77, v16\n"
      "v_perm_b32 v112, v93, v77, v20\n"
      "v_perm_b32 v128, v94, v78, v16\n"
      "v_perm_b32 v144, v94, v78, v20\n"
      "v_perm_b32 v160, v95, v79, v16\n"
      "v_perm_b32 v176, v95, v79, v20\n"
      "ds_read_b128 v[24:27], v15 offset:1664\n"
      "ds_read_b128 v[28:31], v15 offset:1872\n"
      "ds_read_b128 v[32:35], v15 offset:2080\n"
      "ds_read_b128 v[36:39], v15 offset:2288\n"
      "v_perm_b32 v65, v124, v108, v17\n"
      "v_perm_b32 v81, v124, v108, v21\n"
      "v_perm_b32 v97, v125, v109, v17\n"
      "v_perm_b32 v113, v125, v109, v21\n"
      "v_perm_b32 v129, v126, v110, v17\n"
      "v_perm_b32 v145, v126, v110, v21\n"
      "v_perm_b32 v161, v127, v111, v17\n"
      "v_perm_b32 v177, v127, v111, v21\n"
      "buffer_load_dwordx4 v[228:231], v3, s[36:39], s98 offen offset:192\n"
      "buffer_load_dwordx4 v[232:235], v3, s[36:39], s98 offen offset:384\n"
      "s_waitcnt lgkmcnt(4)\n"
      "s_nop 0\n"
      "v_perm_b32 v66, v156, v140, v18\n"
      "v_perm_b32 v82, v156, v140, v22\n"
      "v_perm_b32 v98, v157, v141, v18\n"
      "v_perm_b32 v114, v157, v141, v22\n"
      "v_perm_b32 v130, v158, v142, v18\n"
      "v_perm_b32 v146, v158, v142, v22\n"
      "v_perm_b32 v162, v159, v143, v18\n"
      "v_perm_b32 v178, v159, v143, v22\n"
      "ds_read_b128 v[40:43], v15 offset:2496\n"
      "ds_read_b128 v[44:47], v15 offset:2704\n"
      "ds_read_b128 v[48:51], v15 offset:2912\n"
      "ds_read_b128 v[52:55], v15 offset:3120\n"
      "ds_read_b128 v[8:11], v1 offset:1024\n"
      "v_perm_b32 v67, v188, v172, v19\n"
      "v_perm_b32 v83, v188, v172, v23\n"
      "v_perm_b32 v99, v189, v173, v19\n"
      "v_perm_b32 v115, v189, v173, v23\n"
      "v_perm_b32 v131, v190, v174, v19\n"
      "v_perm_b32 v147, v190, v174, v23\n"
      "v_perm_b32 v163, v191, v175, v19\n"
      "v_perm_b32 v179, v191, v175, v23\n"
      "buffer_load_dwordx4 v[236:239], v4, s[36:39], s98 offen\n"
      "s_mul_i32 s59, s59, 48\n"
      "s_mul_i32 s86, s86, 48\n"
      "s_mul_i32 s61, s96, 48\n"
      "s_nop 0\n"
      "s_mov_b32 s47, 0x20000\n"
      "s_mov_b32 s51, 0x20000\n"
      "s_mov_b32 s50, 9633792\n"
      "s_mov_b32 s46, 73728\n"
      "s_mul_i32 s58, s56, 224\n"
      "s_add_u32 s58, s58, s57\n"
      "s_mul_i32 s58, s58, 192\n"
      "s_cmp_lt_u32 s96, 4\n"
      "s_cselect_b32 s44, s52, s54\n"
      "s_cselect_b32 s45, s53, s55\n"
      "s_mov_b32 s40, 0x05040100\n"
      "s_mov_b32 s41, 0x07060302\n"
      "s_mov_b32 s52, 0x409d30fc\n"
      "s_mov_b32 s53, s52\n"
      "s_mov_b32 s54, 0xbeeff558\n"
      "s_mov_b32 s55, s54\n"
      "s_waitcnt lgkmcnt(5)\n"
      "s_nop 0\n"
      "v_perm_b32 v68, v28, v24, v16\n"
      "v_perm_b32 v84, v28, v24, v20\n"
      "v_perm_b32 v100, v29, v25, v16\n"
      "v_perm_b32 v116, v29, v25, v20\n"
      "v_perm_b32 v132, v30, v26, v16\n"
      "v_perm_b32 v148, v30, v26, v20\n"
      "v_perm_b32 v164, v31, v27, v16\n"
      "v_perm_b32 v180, v31, v27, v20\n"
      "ds_read_b128 v[76:79], v15 offset:3328\n"
      "ds_read_b128 v[92:95], v15 offset:3536\n"
      "ds_read_b128 v[108:111], v15 offset:3744\n"
      "ds_read_b128 v[124:127], v15 offset:3952\n"
      "v_perm_b32 v69, v36, v32, v17\n"
      "v_perm_b32 v85, v36, v32, v21\n"
      "v_perm_b32 v101, v37, v33, v17\n"
      "v_perm_b32 v117, v37, v33, v21\n"
      "v_perm_b32 v133, v38, v34, v17\n"
      "v_perm_b32 v149, v38, v34, v21\n"
      "v_perm_b32 v165, v39, v35, v17\n"
      "v_perm_b32 v181, v39, v35, v21\n"
      "s_cbranch_vccz .Lcnx_l3\n"
      "buffer_load_dwordx4 v[240:243], v2, s[36:39], s99 offen\n"
      "buffer_load_dwordx4 v[244:247], v3, s[36:39], s99 offen offset:192\n"
      ".Lcnx_l3:\n"
      "s_waitcnt lgkmcnt(5)\n"
      "v_perm_b32 v70, v44, v40, v18\n"
      "v_perm_b32 v86, v44, v40, v22\n"
      "v_perm_b32 v102, v45, v41, v18\n"
      "v_perm_b32 v118, v45, v41, v22\n"
      "v_perm_b32 v134, v46, v42, v18\n"
      "v_perm_b32 v150, v46, v42, v22\n"
      "v_perm_b32 v166, v47, v43, v18\n"
      "v_perm_b32 v182, v47, v43, v22\n"
      "ds_read_b128 v[140:143], v15 offset:4160\n"
      "ds_read_b128 v[156:159], v15 offset:4368\n"
      "ds_read_b128 v[172:175], v15 offset:4576\n"
      "ds_read_b128 v[188:191], v15 offset:4784\n"
      "v_perm_b32 v71, v52, v48, v19\n"
      "v_perm_b32 v87, v52, v48, v23\n"
      "v_perm_b32 v103, v53, v49, v19\n"
      "v_perm_b32 v119, v53, v49, v23\n"
      "v_perm_b32 v135, v54, v50, v19\n"
      "v_perm_b32 v151, v54, v50, v23\n"
      "v_perm_b32 v167, v55, v51, v19\n"
      "v_perm_b32 v183, v55, v51, v23\n"
      "s_cbranch_vccz .Lcnx_l4\n"
      "buffer_load_dwordx4 v[248:251], v3, s[36:39], s99 offen offset:384\n"
      "buffer_load_dwordx4 v[252:255], v4, s[36:39], s99 offen\n"
      ".Lcnx_l4:\n"
      "s_setprio 2\n"
      "s_waitcnt lgkmcnt(4)\n"
      "s_nop 0\n"
      "v_perm_b32 v72, v92, v76, v16\n"
      "v_perm_b32 v88, v92, v76, v20\n"
      "v_perm_b32 v104, v93, v77, v16\n"
      "v_perm_b32 v120, v93, v77, v20\n"
      "v_perm_b32 v136, v94, v78, v16\n"
      "v_perm_b32 v152, v94, v78, v20\n"
      "v_perm_b32 v168, v95, v79, v16\n"
      "v_perm_b32 v184, v95, v79, v20\n"
      "ds_read_b128 v[192:195], v15 offset:4992\n"
      "ds_read_b128 v[196:199], v15 offset:5200\n"
      "ds_read_b128 v[200:203], v15 offset:5408\n"
      "ds_read_b128 v[204:207], v15 offset:5616\n"
      "v_perm_b32 v73, v124, v108, v17\n"
      "v_perm_b32 v89, v124, v108, v21\n"
      "v_perm_b32 v105, v125, v109, v17\n"
      "v_perm_b32 v121, v125, v109, v21\n"
      "v_perm_b32 v137, v126, v110, v17\n"
      "v_perm_b32 v153, v126, v110, v21\n"
      "v_perm_b32 v169, v127, v111, v17\n"
      "v_perm_b32 v185, v127, v111, v21\n"
      "v_add_u32 v12, s61, v11\n"
      "v_add_u32 v13, s86, v11\n"
      "v_add_u32 v14, s59, v11\n"
      "v_cndmask_b32 v7, v6, v10, vcc\n"
      "v_add_u32 v10, s82, v9\n"
      "v_add_u32 v11, s83, v9\n"
      "v_add3_u32 v9, s81, v8, v0\n"
      "s_waitcnt lgkmcnt(4)\n"
      "s_nop 0\n"
      "v_perm_b32 v74, v156, v140, v18\n"
      "v_perm_b32 v90, v156, v140, v22\n"
      "v_perm_b32 v106, v157, v141, v18\n"
      "v_perm_b32 v122, v157, v141, v22\n"
      "v_perm_b32 v138, v158, v142, v18\n"
      "v_perm_b32 v154, v158, v142, v22\n"
      "v_perm_b32 v170, v159, v143, v18\n"
      "v_perm_b32 v186, v159, v143, v22\n"
      "ds_read_b128 v[40:43], v15 offset:5824\n"
      "ds_read_b128 v[44:47], v15 offset:6032\n"
      "ds_read_b128 v[48:51], v15 offset:6240\n"
      "ds_read_b128 v[52:55], v15 offset:6448\n"
      "v_perm_b32 v75, v188, v172, v19\n"
      "v_perm_b32 v91, v188, v172, v23\n"
      "v_perm_b32 v107, v189, v173, v19\n"
      "v_perm_b32 v123, v189, v173, v23\n"
      "v_perm_b32 v139, v190, v174, v19\n"
      "v_perm_b32 v155, v190, v174, v23\n"
      "v_perm_b32 v171, v191, v175, v19\n"
      "v_perm_b32 v187, v191, v175, v23\n"
      "s_waitcnt lgkmcnt(4)\n"
      "s_nop 0\n"
      "v_perm_b32 v76, v196, v192, v16\n"
      "v_perm_b32 v92, v196, v192, v20\n"
      "v_perm_b32 v108, v197, v193, v16\n"
      "v_perm_b32 v124, v197, v193, v20\n"
      "v_perm_b32 v140, v198, v194, v16\n"
      "v_perm_b32 v156, v198, v194, v20\n"
      "v_perm_b32 v172, v199, v195, v16\n"
      "v_perm_b32 v188, v199, v195, v20\n"
      "ds_read_b64 v[24:25], v9 offset:0\n"
      "ds_read_b64 v[26:27], v9 offset:208\n"
      "ds_read_b64 v[28:29], v9 offset:416\n"
      "ds_read_b64 v[30:31], v9 offset:624\n"
      "ds_read_b64 v[32:33], v9 offset:832\n"
      "ds_read_b64 v[34:35], v9 offset:1040\n"
      "ds_read_b64 v[36:37], v9 offset:1248\n"
      "ds_read_b64 v[38:39], v9 offset:1456\n"
      "v_perm_b32 v77, v204, v200, v17\n"
      "v_perm_b32 v93, v204, v200, v21\n"
      "v_perm_b32 v109, v205, v201, v17\n"
      "v_perm_b32 v125, v205, v201, v21\n"
      "v_perm_b32 v141, v206, v202, v17\n"
      "v_perm_b32 v157, v206, v202, v21\n"
      "v_perm_b32 v173, v207, v203, v17\n"
      "v_perm_b32 v189, v207, v203, v21\n"
      "s_waitcnt lgkmcnt(8)\n"
      "s_nop 0\n"
      "v_perm_b32 v78, v44, v40, v18\n"
      "v_perm_b32 v94, v44, v40, v22\n"
      "v_perm_b32 v110, v45, v41, v18\n"
      "v_perm_b32 v126, v45, v41, v22\n"
      "v_perm_b32 v142, v46, v42, v18\n"
      "v_perm_b32 v158, v46, v42, v22\n"
      "v_perm_b32 v174, v47, v43, v18\n"
      "v_perm_b32 v190, v47, v43, v22\n"
      "v_perm_b32 v79, v52, v48, v19\n"
      "v_perm_b32 v95, v52, v48, v23\n"
      "v_perm_b32 v111, v53, v49, v19\n"
      "v_perm_b32 v127, v53, v49, v23\n"
      "v_perm_b32 v143, v54, v50, v19\n"
      "v_perm_b32 v159, v54, v50, v23\n"
      "v_perm_b32 v175, v55, v51, v19\n"
      "v_perm_b32 v191, v55, v51, v23\n"
      "s_setprio 1\n"
      "s_waitcnt lgkmcnt(0)\n"
      "v_perm_b32 v192, v26, v24, v16\n"
      "v_perm_b32 v196, v26, v24, v20\n"
      "v_perm_b32 v200, v27, v25, v16\n"
      "v_perm_b32 v204, v27, v25, v20\n"
      "v_perm_b32 v193, v30, v28, v17\n"
      "v_perm_b32 v197, v30, v28, v21\n"
      "v_perm_b32 v201, v31, v29, v17\n"
      "v_perm_b32 v205, v31, v29, v21\n"
      "v_perm_b32 v194, v34, v32, v18\n"
      "v_perm_b32 v198, v34, v32, v22\n"
      "v_perm_b32 v202, v35, v33, v18\n"
      "v_perm_b32 v206, v35, v33, v22\n"
      "v_perm_b32 v195, v38, v36, v19\n"
      "v_perm_b32 v199, v38, v36, v23\n"
      "v_perm_b32 v203, v39, v37, v19\n"
      "v_perm_b32 v207, v39, v37, v23\n"
      "ds_read_b64 v[24:25], v9 offset:1664\n"
      "ds_read_b64 v[26:27], v9 offset:1872\n"
      "ds_read_b64 v[28:29], v9 offset:2080\n"
      "ds_read_b64 v[30:31], v9 offset:2288\n"
      "ds_read_b64 v[32:33], v9 offset:2496\n"
      "ds_read_b64 v[34:35], v9 offset:2704\n"
      "ds_read_b64 v[36:37], v9 offset:2912\n"
      "ds_read_b64 v[38:39], v9 offset:3120\n"
      "s_waitcnt vmcnt(8)\n"
      "s_cbranch_vccnz .Lcnx_l5\n"
      "s_waitcnt vmcnt(4)\n"
      "s_nop 0\n"
      ".Lcnx_l5:\n"
      "v_perm_b32 v40, v212, v208, s40\n"
      "v_perm_b32 v41, v220, v216, s40\n"
      "v_perm_b32 v42, v212, v208, s41\n"
      "v_perm_b32 v43, v220, v216, s41\n"
      "v_perm_b32 v44, v213, v209, s40\n"
      "v_perm_b32 v45, v221, v217, s40\n"
      "v_perm_b32 v46, v213, v209, s41\n"
      "v_perm_b32 v47, v221, v217, s41\n"
      "ds_write_b64 v12, v[40:41] offset:16528\n"
      "ds_write_b64 v12, v[42:43] offset:17488\n"
      "v_perm_b32 v48, v214, v210, s40\n"
      "v_perm_b32 v49, v222, v218, s40\n"
      "v_perm_b32 v50, v214, v210, s41\n"
      "v_perm_b32 v51, v222, v218, s41\n"
      "v_perm_b32 v52, v215, v211, s40\n"
      "v_perm_b32 v53, v223, v219, s40\n"
      "v_perm_b32 v54, v215, v211, s41\n"
      "v_perm_b32 v55, v223, v219, s41\n"
      "ds_write_b64 v12, v[44:45] offset:18448\n"
      "ds_write_b64 v12, v[46:47] offset:19408\n"
      "s_waitcnt lgkmcnt(4)\n"
      "s_nop 0\n"
      "v_perm_b32 v208, v26, v24, v16\n"
      "v_perm_b32 v212, v26, v24, v20\n"
      "v_perm_b32 v216, v27, v25, v16\n"
      "v_perm_b32 v220, v27, v25, v20\n"
      "v_perm_b32 v209, v30, v28, v17\n"
      "v_perm_b32 v213, v30, v28, v21\n"
      "v_perm_b32 v217, v31, v29, v17\n"
      "v_perm_b32 v221, v31, v29, v21\n"
      "ds_read_b64 v[24:25], v9 offset:3328\n"
      "ds_read_b64 v[26:27], v9 offset:3536\n"
      "ds_read_b64 v[28:29], v9 offset:3744\n"
      "ds_read_b64 v[30:31], v9 offset:3952\n"
      "ds_write_b64 v12, v[48:49] offset:20368\n"
      "ds_write_b64 v12, v[50:51] offset:21328\n"
      "v_perm_b32 v210, v34, v32, v18\n"
      "v_perm_b32 v214, v34, v32, v22\n"
      "v_perm_b32 v218, v35, v33, v18\n"
      "v_perm_b32 v222, v35, v33, v22\n"
      "v_perm_b32 v211, v38, v36, v19\n"
      "v_perm_b32 v215, v38, v36, v23\n"
      "v_perm_b32 v219, v39, v37, v19\n"
      "v_perm_b32 v223, v39, v37, v23\n"
      "ds_read_b64 v[32:33], v9 offset:4160\n"
      "ds_read_b64 v[34:35], v9 offset:4368\n"
      "ds_read_b64 v[36:37], v9 offset:4576\n"
      "ds_read_b64 v[38:39], v9 offset:4784\n"
      "ds_write_b64 v12, v[52:53] offset:22288\n"
      "ds_write_b64 v12, v[54:55] offset:23248\n"
      "s_waitcnt vmcnt(4)\n"
      "s_cbranch_vccnz .Lcnx_l6\n"
      "s_waitcnt vmcnt(0)\n"
      ".Lcnx_l6:\n"
      "s_setprio 0\n"
      "v_perm_b32 v40, v228, v224, s40\n"
      "v_perm_b32 v41, v236, v232, s40\n"
      "v_perm_b32 v42, v228, v224, s41\n"
      "v_perm_b32 v43, v236, v232, s41\n"
      "v_perm_b32 v44, v229, v225, s40\n"
      "v_perm_b32 v45, v237, v233, s40\n"
      "v_perm_b32 v46, v229, v225, s41\n"
      "v_perm_b32 v47, v237, v233, s41\n"
      "ds_write_b64 v13, v[40:41] offset:16384\n"
      "ds_write_b64 v13, v[42:43] offset:17344\n"
      "v_perm_b32 v48, v230, v226, s40\n"
      "v_perm_b32 v49, v238, v234, s40\n"
      "v_perm_b32 v50, v230, v226, s41\n"
      "v_perm_b32 v51, v238, v234, s41\n"
      "v_perm_b32 v52, v231, v227, s40\n"
      "v_perm_b32 v53, v239, v235, s40\n"
      "v_perm_b32 v54, v231, v227, s41\n"
      "v_perm_b32 v55, v239, v235, s41\n"
      "ds_write_b64 v13, v[44:45] offset:18304\n"
      "ds_write_b64 v13, v[46:47] offset:19264\n"
      "s_waitcnt lgkmcnt(4)\n"
      "s_nop 0\n"
      "v_perm_b32 v224, v26, v24, v16\n"
      "v_perm_b32 v228, v26, v24, v20\n"
      "v_perm_b32 v232, v27, v25, v16\n"
      "v_perm_b32 v236, v27, v25, v20\n"
      "v_perm_b32 v225, v30, v28, v17\n"
      "v_perm_b32 v229, v30, v28, v21\n"
      "v_perm_b32 v233, v31, v29, v17\n"
      "v_perm_b32 v237, v31, v29, v21\n"
      "ds_read_b64 v[24:25], v9 offset:4992\n"
      "ds_read_b64 v[26:27], v9 offset:5200\n"
      "ds_read_b64 v[28:29], v9 offset:5408\n"
      "ds_read_b64 v[30:31], v9 offset:5616\n"
      "ds_write_b64 v13, v[48:49] offset:20224\n"
      "ds_write_b64 v13, v[50:51] offset:21184\n"
      "v_perm_b32 v226, v34, v32, v18\n"
      "v_perm_b32 v230, v34, v32, v22\n"
      "v_perm_b32 v234, v35, v33, v18\n"
      "v_perm_b32 v238, v35, v33, v22\n"
      "v_perm_b32 v227, v38, v36, v19\n"
      "v_perm_b32 v231, v38, v36, v23\n"
      "v_perm_b32 v235, v39, v37, v19\n"
      "v_perm_b32 v239, v39, v37, v23\n"
      "ds_read_b64 v[32:33], v9 offset:5824\n"
      "ds_read_b64 v[34:35], v9 offset:6032\n"
      "ds_read_b64 v[36:37], v9 offset:6240\n"
      "ds_read_b64 v[38:39], v9 offset:6448\n"
      "ds_write_b64 v13, v[52:53] offset:22144\n"
      "ds_write_b64 v13, v[54:55] offset:23104\n"
      "s_cbranch_vccz .Lcnx_st3\n"
      "s_waitcnt vmcnt(0)\n"
      "v_perm_b32 v40, v244, v240, s40\n"
      "v_perm_b32 v41, v252, v248, s40\n"
      "v_perm_b32 v42, v244, v240, s41\n"
      "v_perm_b32 v43, v252, v248, s41\n"
      "v_perm_b32 v44, v245, v241, s40\n"
      "v_perm_b32 v45, v253, v249, s40\n"
      "v_perm_b32 v46, v245, v241, s41\n"
      "v_perm_b32 v47, v253, v249, s41\n"
      "ds_write_b64 v14, v[40:41] offset:16384\n"
      "ds_write_b64 v14, v[42:43] offset:17344\n"
      "v_perm_b32 v48, v246, v242, s40\n"
      "v_perm_b32 v49, v254, v250, s40\n"
      "v_perm_b32 v50, v246, v242, s41\n"
      "v_perm_b32 v51, v254, v250, s41\n"
      "v_perm_b32 v52, v247, v243, s40\n"
      "v_perm_b32 v53, v255, v251, s40\n"
      "v_perm_b32 v54, v247, v243, s41\n"
      "v_perm_b32 v55, v255, v251, s41\n"
      "ds_write_b64 v14, v[44:45] offset:18304\n"
      "ds_write_b64 v14, v[46:47] offset:19264\n"
      "s_waitcnt lgkmcnt(4)\n"
      "s_nop 0\n"
      "v_perm_b32 v240, v26, v24, v16\n"
      "v_perm_b32 v244, v26, v24, v20\n"
      "v_perm_b32 v248, v27, v25, v16\n"
      "v_perm_b32 v252, v27, v25, v20\n"
      "v_perm_b32 v241, v30, v28, v17\n"
      "v_perm_b32 v245, v30, v28, v21\n"
      "v_perm_b32 v249, v31, v29, v17\n"
      "v_perm_b32 v253, v31, v29, v21\n"
      "ds_write_b64 v14, v[48:49] offset:20224\n"
      "ds_write_b64 v14, v[50:51] offset:21184\n"
      "v_perm_b32 v242, v34, v32, v18\n"
      "v_perm_b32 v246, v34, v32, v22\n"
      "v_perm_b32 v250, v35, v33, v18\n"
      "v_perm_b32 v254, v35, v33, v22\n"
      "v_perm_b32 v243, v38, v36, v19\n"
      "v_perm_b32 v247, v38, v36, v23\n"
      "v_perm_b32 v251, v39, v37, v19\n"
      "v_perm_b32 v255, v39, v37, v23\n"
      "ds_write_b64 v14, v[52:53] offset:22144\n"
      "ds_write_b64 v14, v[54:55] offset:23104\n"
      "s_branch .Lcnx_st4\n"
      ".Lcnx_st3:\n"
      "s_waitcnt lgkmcnt(2)\n"
      "v_perm_b32 v240, v26, v24, v16\n"
      "v_perm_b32 v244, v26, v24, v20\n"
      "v_perm_b32 v248, v27, v25, v16\n"
      "v_perm_b32 v252, v27, v25, v20\n"
      "v_perm_b32 v241, v30, v28, v17\n"
      "v_perm_b32 v245, v30, v28, v21\n"
      "v_perm_b32 v249, v31, v29, v17\n"
      "v_perm_b32 v253, v31, v29, v21\n"
      "v_perm_b32 v242, v34, v32, v18\n"
      "v_perm_b32 v246, v34, v32, v22\n"
      "v_perm_b32 v250, v35, v33, v18\n"
      "v_perm_b32 v254, v35, v33, v22\n"
      "v_perm_b32 v243, v38, v36, v19\n"
      "v_perm_b32 v247, v38, v36, v23\n"
      "v_perm_b32 v251, v39, v37, v19\n"
      "v_perm_b32 v255, v39, v37, v23\n"
      ".Lcnx_st4:\n"
      "v_add_u32 v9, s95, v6\n"
      "v_add_u32_e64 v8, s79, v6\n"
      "s_setprio 3\n"
      "s_waitcnt vmcnt(0) lgkmcnt(0)\n"
      "s_barrier\n"
      "ds_read_b128 v[0:3], v1\n"
      "ds_read_b128 v[24:27], v10 offset:0\n"
      "ds_read_b128 v[28:31], v10 offset:48\n"
      "ds_read_b128 v[32:35], v10 offset:96\n"
      "ds_read_b128 v[36:39], v10 offset:144\n"
      "ds_read_b128 v[40:43], v10 offset:960\n"
      "ds_read_b128 v[44:47], v10 offset:1008\n"
      "ds_read_b128 v[48:51], v10 offset:1056\n"
      "ds_read_b128 v[52:55], v10 offset:1104\n"
      "s_waitcnt lgkmcnt(6)\n"
      "s_nop 0\n"
      "v_mfma_f32_16x16x32_bf16 v[12:15], v[24:27], v[64:67], 0\n"
      "ds_read_b128 v[24:27], v10 offset:1920\n"
      "v_mfma_f32_16x16x32_bf16 v[12:15], v[28:31], v[68:71], v[12:15]\n"
      "ds_read_b128 v[28:31], v10 offset:1968\n"
      "s_cbranch_vccz .Lcnx_l7\n"
      "s_mov_b32 m0, s67\n"
      "s_add_u32 s98, s64, 5120\n"
      "buffer_load_dwordx4 v7, s[44:47], s64 offen lds\n"
      "buffer_load_dwordx4 v7, s[44:47], s98 offen offset:1024 lds\n"
      "s_addk_i32 s98, 5120\n"
      "s_addk_i32 s64, 128\n"
      ".Lcnx_l7:\n"
      "s_waitcnt lgkmcnt(6)\n"
      "v_mfma_f32_16x16x32_bf16 v[12:15], v[32:35], v[72:75], v[12:15]\n"
      "ds_read_b128 v[32:35], v10 offset:2016\n"
      "v_add_u32 v2, s76, v2\n"
      "v_add_u32 v0, s77, v0\n"
      "v_mfma_f32_16x16x32_bf16 v[12:15], v[36:39], v[76:79], v[12:15]\n"
      "ds_read_b128 v[36:39], v10 offset:2064\n"
      "v_add_u32 v1, s78, v1\n"
      "v_add_u32 v16, s84, v3\n"
      "s_setprio 2\n"
      "s_waitcnt lgkmcnt(6)\n"
      "v_mfma_f32_16x16x32_bf16 v[64:67], v[40:43], v[80:83], 0\n"
      "ds_read_b128 v[40:43], v10 offset:2880\n"
      "v_add_u32 v17, s85, v3\n"
      "v_add_u32 v4, s69, v0\n"
      "v_lshrrev_b32 v18, 2, v8\n"
      "s_mov_b32 s42, 0x3c2aaaab\n"
      "s_mov_b32 s43, 0x3727c5ac\n"
      "v_mfma_f32_16x16x32_bf16 v[64:67], v[44:47], v[84:87], v[64:67]\n"
      "ds_read_b128 v[44:47], v10 offset:2928\n"
      "v_xor_b32 v21, 32, v6\n"
      "v_mov_b32 v19, v6\n"
      "s_waitcnt lgkmcnt(6)\n"
      "v_mfma_f32_16x16x32_bf16 v[64:67], v[48:51], v[88:91], v[64:67]\n"
      "ds_read_b128 v[48:51], v10 offset:2976\n"
      "v_mov_b32 v20, v6\n"
      "v_mov_b32_dpp v19, v21 quad_perm:[0,1,2,3] row_mask:0x3 bank_mask:0xf\n"
      "v_mfma_f32_16x16x32_bf16 v[64:67], v[52:55], v[92:95], v[64:67]\n"
      "ds_read_b128 v[52:55], v10 offset:3024\n"
      "v_mov_b32_dpp v20, v21 quad_perm:[0,1,2,3] row_mask:0xe bank_mask:0xf\n"
      "s_cbranch_vccnz .Lcnx_l8\n"
      "v_mov_b32_dpp v7, v21 quad_perm:[0,1,2,3] row_mask:0x8 bank_mask:0xf\n"
      ".Lcnx_l8:\n"
      "s_waitcnt lgkmcnt(6)\n"
      "v_mfma_f32_16x16x32_bf16 v[68:71], v[24:27], v[96:99], 0\n"
      "ds_read_b128 v[24:27], v10 offset:3840\n"
      "v_add_f32 v56, v56, v12\n"
      "v_mul_f32 v60, v12, v12\n"
      "v_mfma_f32_16x16x32_bf16 v[68:71], v[28:31], v[100:103], v[68:71]\n"
      "ds_read_b128 v[28:31], v10 offset:3888\n"
      "s_cbranch_vccz .Lcnx_l9\n"
      "buffer_load_dwordx4 v7, s[44:47], s98 offen offset:2048 lds\n"
      "s_addk_i32 s67, -24576\n"
      "s_mov_b32 m0, s67\n"
      "s_add_u32 s98, s64, 5120\n"
      "buffer_load_dwordx4 v7, s[44:47], s64 offen lds\n"
      ".Lcnx_l9:\n"
      "v_add_f32 v57, v57, v13\n"
      "v_mul_f32 v61, v13, v13\n"
      "s_waitcnt lgkmcnt(6)\n"
      "v_mfma_f32_16x16x32_bf16 v[68:71], v[32:35], v[104:107], v[68:71]\n"
      "ds_read_b128 v[32:35], v10 offset:3936\n"
      "v_add_f32 v58, v58, v14\n"
      "v_mul_f32 v62, v14, v14\n"
      "v_mfma_f32_16x16x32_bf16 v[68:71], v[36:39], v[108:111], v[68:71]\n"
      "ds_read_b128 v[36:39], v10 offset:3984\n"
      "v_add_f32 v59, v59, v15\n"
      "v_mul_f32 v63, v15, v15\n"
      "s_waitcnt lgkmcnt(6)\n"
      "v_mfma_f32_16x16x32_bf16 v[72:75], v[40:43], v[112:115], 0\n"
      "ds_read_b128 v[40:43], v10 offset:4800\n"
      "v_add_f32 v56, v56, v64\n"
      "v_fmac_f32 v60, v64, v64\n"
      "v_mfma_f32_16x16x32_bf16 v[72:75], v[44:47], v[116:119], v[72:75]\n"
      "ds_read_b128 v[44:47], v10 offset:4848\n"
      "v_add_f32 v57, v57, v65\n"
      "v_fmac_f32 v61, v65, v65\n"
      "s_waitcnt lgkmcnt(6)\n"
      "v_mfma_f32_16x16x32_bf16 v[72:75], v[48:51], v[120:123], v[72:75]\n"
      "ds_read_b128 v[48:51], v10 offset:4896\n"
      "v_add_f32 v58, v58, v66\n"
      "v_fmac_f32 v62, v66, v66\n"
      "v_mfma_f32_16x16x32_bf16 v[72:75], v[52:55], v[124:127], v[72:75]\n"
      "ds_read_b128 v[52:55], v10 offset:4944\n"
      "v_add_f32 v59, v59, v67\n"
      "v_fmac_f32 v63, v67, v67\n"
      "s_waitcnt lgkmcnt(6)\n"
      "v_mfma_f32_16x16x32_bf16 v[76:79], v[24:27], v[128:131], 0\n"
      "ds_read_b128 v[24:27], v10 offset:5760\n"
      "v_add_f32 v56, v56, v68\n"
      "v_fmac_f32 v60, v68, v68\n"
      "v_mfma_f32_16x16x32_bf16 v[76:79], v[28:31], v[132:135], v[76:79]\n"
      "ds_read_b128 v[28:31], v10 offset:5808\n"
      "s_cbranch_vccz .Lcnx_l10\n"
      "buffer_load_dwordx4 v7, s[44:47], s98 offen offset:1024 lds\n"
      "s_addk_i32 s98, 5120\n"
      "s_addk_i32 s64, 128\n"
      "buffer_load_dwordx4 v7, s[44:47], s98 offen offset:2048 lds\n"
      "s_addk_i32 s67, -24576\n"
      ".Lcnx_l10:\n"
      "s_setprio 1\n"
      "v_add_f32 v57, v57, v69\n"
      "v_fmac_f32 v61, v69, v69\n"
      "s_waitcnt lgkmcnt(6)\n"
      "v_mfma_f32_16x16x32_bf16 v[76:79], v[32:35], v[136:139], v[76:79]\n"
      "ds_read_b128 v[32:35], v10 offset:5856\n"
      "v_add_f32 v58, v58, v70\n"
      "v_fmac_f32 v62, v70, v70\n"
      "v_mfma_f32_16x16x32_bf16 v[76:79], v[36:39], v[140:143], v[76:79]\n"
      "ds_read_b128 v[36:39], v10 offset:5904\n"
      "v_add_f32 v59, v59, v71\n"
      "v_fmac_f32 v63, v71, v71\n"
      "s_waitcnt lgkmcnt(6)\n"
      "v_mfma_f32_16x16x32_bf16 v[80:83], v[40:43], v[144:147], 0\n"
      "ds_read_b128 v[40:43], v10 offset:6720\n"
      "v_add_f32 v56, v56, v72\n"
      "v_fmac_f32 v60, v72, v72\n"
      "v_mfma_f32_16x16x32_bf16 v[80:83], v[44:47], v[148:151], v[80:83]\n"
      "ds_read_b128 v[44:47], v10 offset:6768\n"
      "v_add_f32 v57, v57, v73\n"
      "v_fmac_f32 v61, v73, v73\n"
      "s_waitcnt lgkmcnt(6)\n"
      "v_mfma_f32_16x16x32_bf16 v[80:83], v[48:51], v[152:155], v[80:83]\n"
      "ds_read_b128 v[48:51], v10 offset:6816\n"
      "v_add_f32 v58, v58, v74\n"
      "v_fmac_f32 v62, v74, v74\n"
      "v_mfma_f32_16x16x32_bf16 v[80:83], v[52:55], v[156:159], v[80:83]\n"
      "ds_read_b128 v[52:55], v10 offset:6864\n"
      "v_add_f32 v59, v59, v75\n"
      "v_fmac_f32 v63, v75, v75\n"
      "s_waitcnt lgkmcnt(6)\n"
      "v_mfma_f32_16x16x32_bf16 v[84:87], v[24:27], v[160:163], 0\n"
      "ds_read_b128 v[24:27], v11 offset:0\n"
      "v_add_f32 v56, v56, v76\n"
      "v_fmac_f32 v60, v76, v76\n"
      "v_mfma_f32_16x16x32_bf16 v[84:87], v[28:31], v[164:167], v[84:87]\n"
      "ds_read_b128 v[28:31], v11 offset:48\n"
      "v_add_f32 v57, v57, v77\n"
      "v_fmac_f32 v61, v77, v77\n"
      "s_waitcnt lgkmcnt(6)\n"
      "v_mfma_f32_16x16x32_bf16 v[84:87], v[32:35], v[168:171], v[84:87]\n"
      "ds_read_b128 v[32:35], v11 offset:96\n"
      "v_add_f32 v58, v58, v78\n"
      "v_fmac_f32 v62, v78, v78\n"
      "v_mfma_f32_16x16x32_bf16 v[84:87], v[36:39], v[172:175], v[84:87]\n"
      "ds_read_b128 v[36:39], v11 offset:144\n"
      "v_add_f32 v59, v59, v79\n"
      "v_fmac_f32 v63, v79, v79\n"
      "s_waitcnt lgkmcnt(6)\n"
      "v_mfma_f32_16x16x32_bf16 v[88:91], v[40:43], v[176:179], 0\n"
      "ds_read_b128 v[40:43], v11 offset:960\n"
      "v_add_f32 v56, v56, v80\n"
      "v_fmac_f32 v60, v80, v80\n"
      "v_mfma_f32_16x16x32_bf16 v[88:91], v[44:47], v[180:183], v[88:91]\n"
      "ds_read_b128 v[44:47], v11 offset:1008\n"
      "v_add_f32 v57, v57, v81\n"
      "v_fmac_f32 v61, v81, v81\n"
      "s_waitcnt lgkmcnt(6)\n"
      "v_mfma_f32_16x16x32_bf16 v[88:91], v[48:51], v[184:187], v[88:91]\n"
      "ds_read_b128 v[48:51], v11 offset:1056\n"
      "v_add_f32 v58, v58, v82\n"
      "v_fmac_f32 v62, v82, v82\n"
      "v_mfma_f32_16x16x32_bf16 v[88:91], v[52:55], v[188:191], v[88:91]\n"
      "ds_read_b128 v[52:55], v11 offset:1104\n"
      "s_setprio 0\n"
      "v_add_f32 v59, v59, v83\n"
      "v_fmac_f32 v63, v83, v83\n"
      "s_waitcnt lgkmcnt(6)\n"
      "v_mfma_f32_16x16x32_bf16 v[92:95], v[24:27], v[192:195], 0\n"
      "ds_read_b128 v[24:27], v11 offset:1920\n"
      "v_add_f32 v56, v56, v84\n"
      "v_fmac_f32 v60, v84, v84\n"
      "v_mfma_f32_16x16x32_bf16 v[92:95], v[28:31], v[208:211], v[92:95]\n"
      "ds_read_b128 v[28:31], v11 offset:1968\n"
      "v_add_f32 v57, v57, v85\n"
      "v_fmac_f32 v61, v85, v85\n"
      "s_waitcnt lgkmcnt(6)\n"
      "v_mfma_f32_16x16x32_bf16 v[92:95], v[32:35], v[224:227], v[92:95]\n"
      "ds_read_b128 v[32:35], v11 offset:2016\n"
      "v_add_f32 v58, v58, v86\n"
      "v_fmac_f32 v62, v86, v86\n"
      "v_mfma_f32_16x16x32_bf16 v[92:95], v[36:39], v[240:243], v[92:95]\n"
      "ds_read_b128 v[36:39], v11 offset:2064\n"
      "v_add_f32 v59, v59, v87\n"
      "v_fmac_f32 v63, v87, v87\n"
      "s_waitcnt lgkmcnt(6)\n"
      "v_mfma_f32_16x16x32_bf16 v[96:99], v[40:43], v[196:199], 0\n"
      "ds_read_b128 v[40:43], v11 offset:2880\n"
      "v_add_f32 v56, v56, v88\n"
      "v_fmac_f32 v60, v88, v88\n"
      "v_mfma_f32_16x16x32_bf16 v[96:99], v[44:47], v[212:215], v[96:99]\n"
      "ds_read_b128 v[44:47], v11 offset:2928\n"
      "v_add_f32 v57, v57, v89\n"
      "v_fmac_f32 v61, v89, v89\n"
      "s_waitcnt lgkmcnt(6)\n"
      "v_mfma_f32_16x16x32_bf16 v[96:99], v[48:51], v[228:231], v[96:99]\n"
      "ds_read_b128 v[48:51], v11 offset:2976\n"
      "v_add_f32 v58, v58, v90\n"
      "v_fmac_f32 v62, v90, v90\n"
      "v_mfma_f32_16x16x32_bf16 v[96:99], v[52:55], v[244:247], v[96:99]\n"
      "ds_read_b128 v[52:55], v11 offset:3024\n"
      "v_add_f32 v59, v59, v91\n"
      "v_fmac_f32 v63, v91, v91\n"
      "s_waitcnt lgkmcnt(6)\n"
      "v_mfma_f32_16x16x32_bf16 v[100:103], v[24:27], v[200:203], 0\n"
      "v_add_f32 v56, v56, v92\n"
      "v_fmac_f32 v60, v92, v92\n"
      "v_mfma_f32_16x16x32_bf16 v[100:103], v[28:31], v[216:219], v[100:103]\n"
      "v_add_f32 v57, v57, v93\n"
      "v_fmac_f32 v61, v93, v93\n"
      "s_waitcnt lgkmcnt(4)\n"
      "v_mfma_f32_16x16x32_bf16 v[100:103], v[32:35], v[232:235], v[100:103]\n"
      "v_add_f32 v58, v58, v94\n"
      "v_fmac_f32 v62, v94, v94\n"
      "v_mfma_f32_16x16x32_bf16 v[100:103], v[36:39], v[248:251], v[100:103]\n"
      "v_add_f32 v59, v59, v95\n"
      "v_fmac_f32 v63, v95, v95\n"
      "s_waitcnt lgkmcnt(2)\n"
      "v_mfma_f32_16x16x32_bf16 v[104:107], v[40:43], v[204:207], 0\n"
      "v_add_f32 v56, v56, v96\n"
      "v_fmac_f32 v60, v96, v96\n"
      "v_mfma_f32_16x16x32_bf16 v[104:107], v[44:47], v[220:223], v[104:107]\n"
      "v_add_f32 v57, v57, v97\n"
      "v_fmac_f32 v61, v97, v97\n"
      "s_waitcnt lgkmcnt(0)\n"
      "v_mfma_f32_16x16x32_bf16 v[104:107], v[48:51], v[236:239], v[104:107]\n"
      "v_add_f32 v58, v58, v98\n"
      "v_fmac_f32 v62, v98, v98\n"
      "v_mfma_f32_16x16x32_bf16 v[104:107], v[52:55], v[252:255], v[104:107]\n"
      "v_add_f32 v59, v59, v99\n"
      "v_fmac_f32 v63, v99, v99\n"
      "v_add_f32 v56, v56, v100\n"
      "v_fmac_f32 v60, v100, v100\n"
      "v_add_f32 v57, v57, v101\n"
      "v_fmac_f32 v61, v101, v101\n"
      "v_add_f32 v58, v58, v102\n"
      "v_fmac_f32 v62, v102, v102\n"
      "v_add_f32 v59, v59, v103\n"
      "v_fmac_f32 v63, v103, v103\n"
      "v_add_f32 v56, v56, v104\n"
      "v_fmac_f32 v60, v104, v104\n"
      "v_add_f32 v57, v57, v105\n"
      "v_fmac_f32 v61, v105, v105\n"
      "v_add_f32 v58, v58, v106\n"
      "v_fmac_f32 v62, v106, v106\n"
      "v_add_f32 v59, v59, v107\n"
      "v_fmac_f32 v63, v107, v107\n"
      "ds_write_b128 v9, v[56:59]\n"
      "ds_write_b128 v9, v[60:63] offset:1024\n"
      "s_waitcnt vmcnt(0) lgkmcnt(0)\n"
      "s_barrier\n"
      "s_cbranch_vccz .Lcnx_lnb\n"
      "s_nop 0\n"
      "ds_read2st64_b32 v[192:193], v18 offset0:0 offset1:4\n"
      "ds_read2st64_b32 v[194:195], v18 offset0:8 offset1:12\n"
      "ds_read2st64_b32 v[196:197], v18 offset0:16 offset1:20\n"
      "ds_read2st64_b32 v[198:199], v18 offset0:24 offset1:28\n"
      "ds_read2st64_b32 v[200:201], v18 offset0:32 offset1:36\n"
      "ds_read2st64_b32 v[202:203], v18 offset0:40 offset1:44\n"
      "ds_read2st64_b32 v[204:205], v18 offset0:48 offset1:52\n"
      "ds_read2st64_b32 v[206:207], v18 offset0:56 offset1:60\n"
      "s_waitcnt lgkmcnt(4)\n"
      "s_nop 0\n"
      "v_pk_add_f32 v[192:193], v[192:193], v[194:195]\n"
      "v_pk_add_f32 v[196:197], v[196:197], v[198:199]\n"
      "v_pk_add_f32 v[192:193], v[192:193], v[196:197]\n"
      "s_waitcnt lgkmcnt(0)\n"
      "s_nop 0\n"
      "v_pk_add_f32 v[200:201], v[200:201], v[202:203]\n"
      "v_pk_add_f32 v[204:205], v[204:205], v[206:207]\n"
      "v_pk_add_f32 v[192:193], v[192:193], v[200:201]\n"
      "v_pk_add_f32 v[192:193], v[192:193], v[204:205]\n"
      "v_mul_f32 v192, s42, v192\n"
      "v_fma_f32 v194, -v192, v192, s43\n"
      "v_fma_f32 v193, v193, s42, v194\n"
      "v_max_f32 v193, s43, v193\n"
      "v_rsq_f32 v193, v193\n"
      "s_nop 0\n"
      "v_mul_f32 v193, s54, v193\n"
      "v_mul_f32_e64 v192, -v192, v193\n"
      "ds_write2st64_b32 v18, v192, v193 offset0:224 offset1:228\n"
      "s_waitcnt lgkmcnt(0)\n"
      "s_barrier\n"
      "s_nop 0\n"
      "ds_read_b128 v[192:195], v6 offset:57344\n"
      "ds_read_b128 v[196:199], v6 offset:58368\n"
      "ds_read_b64_tr_b16 v[160:161], v0 offset:36864\n"
      "ds_read_b64_tr_b16 v[162:163], v0 offset:37888\n"
      "ds_read_b64_tr_b16 v[164:165], v0 offset:38912\n"
      "ds_read_b64_tr_b16 v[166:167], v0 offset:39936\n"
      "ds_read_b64_tr_b16 v[168:169], v0 offset:40960\n"
      "ds_read_b64_tr_b16 v[170:171], v0 offset:41984\n"
      "ds_read_b64_tr_b16 v[172:173], v0 offset:43008\n"
      "ds_read_b64_tr_b16 v[174:175], v0 offset:44032\n"
      "ds_read_b64_tr_b16 v[176:177], v0 offset:45056\n"
      "ds_read_b64_tr_b16 v[178:179], v0 offset:46080\n"
      "ds_read_b64_tr_b16 v[180:181], v0 offset:47104\n"
      "ds_read_b64_tr_b16 v[182:183], v0 offset:48128\n"
      "s_waitcnt lgkmcnt(12)\n"
      "s_branch .Lcnx_lnc\n"
      ".Lcnx_lnb:\n"
      "s_sleep 1\n"
      "s_nop 0\n"
      "ds_read_b64_tr_b16 v[160:161], v0 offset:36864\n"
      "ds_read_b64_tr_b16 v[162:163], v0 offset:37888\n"
      "ds_read_b64_tr_b16 v[164:165], v0 offset:38912\n"
      "ds_read_b64_tr_b16 v[166:167], v0 offset:39936\n"
      "ds_read_b64_tr_b16 v[168:169], v0 offset:40960\n"
      "ds_read_b64_tr_b16 v[170:171], v0 offset:41984\n"
      "ds_read_b64_tr_b16 v[172:173], v0 offset:43008\n"
      "ds_read_b64_tr_b16 v[174:175], v0 offset:44032\n"
      "ds_read_b64_tr_b16 v[176:177], v0 offset:45056\n"
      "ds_read_b64_tr_b16 v[178:179], v0 offset:46080\n"
      "ds_read_b64_tr_b16 v[180:181], v0 offset:47104\n"
      "ds_read_b64_tr_b16 v[182:183], v0 offset:48128\n"
      "ds_read_b64_tr_b16 v[136:137], v4 offset:12288\n"
      "ds_read_b64_tr_b16 v[138:139], v4 offset:13312\n"
      "ds_read_b64_tr_b16 v[140:141], v4 offset:14336\n"
      "ds_read_b64_tr_b16 v[142:143], v4 offset:15360\n"
      "ds_read_b64_tr_b16 v[144:145], v4 offset:16384\n"
      "ds_read_b64_tr_b16 v[146:147], v4 offset:17408\n"
      "ds_read_b64_tr_b16 v[148:149], v4 offset:18432\n"
      "ds_read_b64_tr_b16 v[150:151], v4 offset:19456\n"
      "ds_read_b64_tr_b16 v[152:153], v4 offset:20480\n"
      "ds_read_b64_tr_b16 v[154:155], v4 offset:21504\n"
      "ds_read_b64_tr_b16 v[156:157], v4 offset:22528\n"
      "ds_read_b64_tr_b16 v[158:159], v4 offset:23552\n"
      "s_mov_b32 m0, s67\n"
      "s_addk_i32 s67, -24576\n"
      "buffer_load_dwordx4 v7, s[44:47], s64 offen lds\n"
      "buffer_load_dwordx4 v19, s[44:47], s64 offen offset:1024 lds\n"
      "buffer_load_dwordx4 v20, s[44:47], s64 offen offset:2048 lds\n"
      "s_addk_i32 s64, 12288\n"
      "s_barrier\n"
      "ds_read_b128 v[192:195], v6 offset:57344\n"
      "ds_read_b128 v[196:199], v6 offset:58368\n"
      "s_waitcnt lgkmcnt(0)\n"
      ".Lcnx_lnc:\n"
      "s_setprio 1\n"
      "v_pk_fma_f32 v[12:13], v[12:13], v[196:197], v[192:193]\n"
      "v_pk_fma_f32 v[64:65], v[64:65], v[196:197], v[192:193]\n"
      "v_pk_fma_f32 v[68:69], v[68:69], v[196:197], v[192:193]\n"
      "v_pk_fma_f32 v[72:73], v[72:73], v[196:197], v[192:193]\n"
      "v_pk_fma_f32 v[76:77], v[76:77], v[196:197], v[192:193]\n"
      "v_pk_fma_f32 v[80:81], v[80:81], v[196:197], v[192:193]\n"
      "v_pk_fma_f32 v[84:85], v[84:85], v[196:197], v[192:193]\n"
      "v_pk_fma_f32 v[88:89], v[88:89], v[196:197], v[192:193]\n"
      "v_pk_fma_f32 v[92:93], v[92:93], v[196:197], v[192:193]\n"
      "v_pk_fma_f32 v[96:97], v[96:97], v[196:197], v[192:193]\n"
      "v_pk_fma_f32 v[100:101], v[100:101], v[196:197], v[192:193]\n"
      "v_pk_fma_f32 v[104:105], v[104:105], v[196:197], v[192:193]\n"
      "v_cvt_pk_bf16_f32 v224, v12, v64\n"
      "v_cvt_pk_bf16_f32 v225, v68, v72\n"
      "v_cvt_pk_bf16_f32 v226, v76, v80\n"
      "v_cvt_pk_bf16_f32 v227, v84, v88\n"
      "v_cvt_pk_bf16_f32 v228, v92, v96\n"
      "v_cvt_pk_bf16_f32 v229, v100, v104\n"
      "v_cvt_pk_bf16_f32 v232, v13, v65\n"
      "v_cvt_pk_bf16_f32 v233, v69, v73\n"
      "v_cvt_pk_bf16_f32 v234, v77, v81\n"
      "v_cvt_pk_bf16_f32 v235, v85, v89\n"
      "v_cvt_pk_bf16_f32 v236, v93, v97\n"
      "v_cvt_pk_bf16_f32 v237, v101, v105\n"
      "ds_write_b128 v16, v[224:227] offset:0\n"
      "ds_write_b64 v17, v[228:229] offset:0\n"
      "s_mov_b32 exec_lo, 0x3f3f3f3f\n"
      "s_mov_b32 exec_hi, 0x3f3f3f3f\n"
      "ds_write_b128 v16, v[232:235] offset:1664\n"
      "ds_write_b64 v17, v[236:237] offset:1664\n"
      "s_mov_b64 exec, -1\n"
      "s_setprio 0\n"
      "v_pk_fma_f32 v[14:15], v[14:15], v[198:199], v[194:195]\n"
      "v_pk_fma_f32 v[66:67], v[66:67], v[198:199], v[194:195]\n"
      "v_pk_fma_f32 v[70:71], v[70:71], v[198:199], v[194:195]\n"
      "v_pk_fma_f32 v[74:75], v[74:75], v[198:199], v[194:195]\n"
      "v_pk_fma_f32 v[78:79], v[78:79], v[198:199], v[194:195]\n"
      "v_pk_fma_f32 v[82:83], v[82:83], v[198:199], v[194:195]\n"
      "v_pk_fma_f32 v[86:87], v[86:87], v[198:199], v[194:195]\n"
      "v_pk_fma_f32 v[90:91], v[90:91], v[198:199], v[194:195]\n"
      "v_pk_fma_f32 v[94:95], v[94:95], v[198:199], v[194:195]\n"
      "v_pk_fma_f32 v[98:99], v[98:99], v[198:199], v[194:195]\n"
      "v_pk_fma_f32 v[102:103], v[102:103], v[198:199], v[194:195]\n"
      "v_pk_fma_f32 v[106:107], v[106:107], v[198:199], v[194:195]\n"
      "v_cvt_pk_bf16_f32 v240, v14, v66\n"
      "v_cvt_pk_bf16_f32 v241, v70, v74\n"
      "v_cvt_pk_bf16_f32 v242, v78, v82\n"
      "v_cvt_pk_bf16_f32 v243, v86, v90\n"
      "v_cvt_pk_bf16_f32 v244, v94, v98\n"
      "v_cvt_pk_bf16_f32 v245, v102, v106\n"
      "v_cvt_pk_bf16_f32 v248, v15, v67\n"
      "v_cvt_pk_bf16_f32 v249, v71, v75\n"
      "v_cvt_pk_bf16_f32 v250, v79, v83\n"
      "v_cvt_pk_bf16_f32 v251, v87, v91\n"
      "v_cvt_pk_bf16_f32 v252, v95, v99\n"
      "v_cvt_pk_bf16_f32 v253, v103, v107\n"
      "s_mov_b32 exec_hi, 0xffff\n"
      "ds_write_b128 v16, v[240:243] offset:5824\n"
      "ds_write_b64 v17, v[244:245] offset:5824\n"
      "s_mov_b32 exec_lo, 0x3f3f3f3f\n"
      "s_mov_b32 exec_hi, 0x3f3f\n"
      "ds_write_b128 v16, v[248:251] offset:7488\n"
      "ds_write_b64 v17, v[252:253] offset:7488\n"
      "s_mov_b64 exec, -1\n"
      "v_add_u32 v5, 19456, v8\n"
      "global_load_dwordx4 v[28:31], v5, %4\n"
      "s_cbranch_vccz .Lcnx_l11\n"
      "s_mov_b32 m0, s67\n"
      "s_nop 0\n"
      "s_add_u32 s98, s64, 5120\n"
      "buffer_load_dwordx4 v7, s[44:47], s64 offen lds\n"
      "buffer_load_dwordx4 v7, s[44:47], s98 offen offset:1024 lds\n"
      "s_addk_i32 s98, 5120\n"
      "s_addk_i32 s64, 128\n"
      "buffer_load_dwordx4 v7, s[44:47], s98 offen offset:2048 lds\n"
      "s_addk_i32 s67, -24576\n"
      "s_mov_b32 m0, s67\n"
      "s_add_u32 s98, s64, 5120\n"
      "buffer_load_dwordx4 v7, s[44:47], s64 offen lds\n"
      "buffer_load_dwordx4 v7, s[44:47], s98 offen offset:1024 lds\n"
      "s_addk_i32 s98, 5120\n"
      "s_addk_i32 s64, 128\n"
      "buffer_load_dwordx4 v7, s[44:47], s98 offen offset:2048 lds\n"
      "s_addk_i32 s67, -24576\n"
      ".Lcnx_l11:\n"
      "s_waitcnt lgkmcnt(0)\n"
      "s_barrier\n"
      "s_cbranch_vccnz .Lcnx_astream\n"
      "ds_read_b128 v[8:11], v6 offset:56320\n"
      "ds_read_b64 v[112:113], v2 offset:0\n"
      "ds_read_b64 v[114:115], v2 offset:16\n"
      "ds_read_b64 v[116:117], v2 offset:32\n"
      "ds_read_b64 v[118:119], v2 offset:48\n"
      "ds_read_b64 v[120:121], v2 offset:64\n"
      "ds_read_b64 v[122:123], v2 offset:80\n"
      "ds_read_b64 v[124:125], v2 offset:96\n"
      "ds_read_b64 v[126:127], v2 offset:112\n"
      "ds_read_b64 v[128:129], v2 offset:128\n"
      "ds_read_b64 v[130:131], v2 offset:144\n"
      "ds_read_b64 v[132:133], v2 offset:160\n"
      "ds_read_b64 v[134:135], v2 offset:176\n"
      "s_mov_b32 exec_hi, 0xfffff\n"
      "s_waitcnt lgkmcnt(12)\n"
      "ds_write_b128 v6, v[8:11] offset:12416\n"
      "s_mov_b32 exec_hi, -1\n"
      "v_mov_b32 v2, v19\n"
      "v_mov_b32 v4, v20\n"
      "s_waitcnt vmcnt(1)\n"
      "s_waitcnt lgkmcnt(0)\n"
      "s_barrier\n"
      "s_setprio 2\n"
      "v_mfma_f32_32x32x16_bf16 v[184:199], v[160:163], v[112:115], 0\n"
      "v_mfma_f32_32x32x16_bf16 v[184:199], v[164:167], v[116:119], v[184:199]\n"
      "v_mfma_f32_32x32x16_bf16 v[184:199], v[168:171], v[120:123], v[184:199]\n"
      "v_mfma_f32_32x32x16_bf16 v[184:199], v[172:175], v[124:127], v[184:199]\n"
      "v_mfma_f32_32x32x16_bf16 v[184:199], v[176:179], v[128:131], v[184:199]\n"
      "v_mfma_f32_32x32x16_bf16 v[184:199], v[180:183], v[132:135], v[184:199]\n"
      "s_nop 11\n"
      "s_nop 0\n"
      "v_pk_fma_f32 v[232:233], v[184:185], v[184:185], s[52:53]\n"
      "v_pk_fma_f32 v[234:235], v[186:187], v[186:187], s[52:53]\n"
      "v_pk_fma_f32 v[236:237], v[188:189], v[188:189], s[52:53]\n"
      "v_pk_fma_f32 v[238:239], v[190:191], v[190:191], s[52:53]\n"
      "v_pk_fma_f32 v[240:241], v[192:193], v[192:193], s[52:53]\n"
      "v_pk_fma_f32 v[242:243], v[194:195], v[194:195], s[52:53]\n"
      "v_pk_fma_f32 v[244:245], v[196:197], v[196:197], s[52:53]\n"
      "v_pk_fma_f32 v[246:247], v[198:199], v[198:199], s[52:53]\n"
      "v_pk_mul_f32 v[232:233], v[232:233], v[184:185]\n"
      "v_pk_mul_f32 v[234:235], v[234:235], v[186:187]\n"
      "v_pk_mul_f32 v[236:237], v[236:237], v[188:189]\n"
      "v_pk_mul_f32 v[238:239], v[238:239], v[190:191]\n"
      "v_pk_mul_f32 v[240:241], v[240:241], v[192:193]\n"
      "v_pk_mul_f32 v[242:243], v[242:243], v[194:195]\n"
      "v_pk_mul_f32 v[244:245], v[244:245], v[196:197]\n"
      "v_pk_mul_f32 v[246:247], v[246:247], v[198:199]\n"
      "v_mfma_f32_32x32x16_bf16 v[200:215], v[136:139], v[112:115], 0\n"
      "v_exp_f32_e64 v232, v232\n"
      "v_exp_f32_e64 v233, v233\n"
      "v_exp_f32_e64 v234, v234\n"
      "v_mfma_f32_32x32x16_bf16 v[200:215], v[140:143], v[116:119], v[200:215]\n"
      "v_exp_f32_e64 v235, v235\n"
      "v_exp_f32_e64 v236, v236\n"
      "v_exp_f32_e64 v237, v237\n"
      "v_mfma_f32_32x32x16_bf16 v[200:215], v[144:147], v[120:123], v[200:215]\n"
      "v_exp_f32_e64 v238, v238\n"
      "v_exp_f32_e64 v239, v239\n"
      "v_exp_f32_e64 v240, v240\n"
      "v_mfma_f32_32x32x16_bf16 v[200:215], v[148:151], v[124:127], v[200:215]\n"
      "v_exp_f32_e64 v241, v241\n"
      "v_exp_f32_e64 v242, v242\n"
      "v_exp_f32_e64 v243, v243\n"
      "v_mfma_f32_32x32x16_bf16 v[200:215], v[152:155], v[128:131], v[200:215]\n"
      "v_exp_f32_e64 v244, v244\n"
      "v_exp_f32_e64 v245, v245\n"
      "v_exp_f32_e64 v246, v246\n"
      "v_mfma_f32_32x32x16_bf16 v[200:215], v[156:159], v[132:135], v[200:215]\n"
      "v_exp_f32_e32 v247, v247\n"
      "s_waitcnt vmcnt(0)\n"
      "v_add_u32 v0, 0xffff4000, v0\n"
      "ds_read_b64_tr_b16 v[136:137], v0 offset:36864\n"
      "ds_read_b64_tr_b16 v[138:139], v0 offset:37888\n"
      "ds_read_b64_tr_b16 v[140:141], v0 offset:38912\n"
      "ds_read_b64_tr_b16 v[142:143], v0 offset:39936\n"
      "ds_read_b64_tr_b16 v[144:145], v0 offset:40960\n"
      "ds_read_b64_tr_b16 v[146:147], v0 offset:41984\n"
      "ds_read_b64_tr_b16 v[148:149], v0 offset:43008\n"
      "ds_read_b64_tr_b16 v[150:151], v0 offset:44032\n"
      "ds_read_b64_tr_b16 v[152:153], v0 offset:45056\n"
      "ds_read_b64_tr_b16 v[154:155], v0 offset:46080\n"
      "ds_read_b64_tr_b16 v[156:157], v0 offset:47104\n"
      "ds_read_b64_tr_b16 v[158:159], v0 offset:48128\n"
      "s_mov_b32 m0, s67\n"
      "s_addk_i32 s67, -24576\n"
      "buffer_load_dwordx4 v7, s[44:47], s64 offen lds\n"
      "buffer_load_dwordx4 v2, s[44:47], s64 offen offset:1024 lds\n"
      "buffer_load_dwordx4 v4, s[44:47], s64 offen offset:2048 lds\n"
      "s_addk_i32 s64, 12288\n"
      "s_and_b32 s97, s96, 3\n"
      "s_min_u32 s97, s97, 2\n"
      "s_lshl_b32 s97, s97, 10\n"
      "s_add_u32 m0, s97, 13312\n"
      "s_add_u32 s97, s97, 4096\n"
      "v_add_u32 v5, s97, v6\n"
      "global_load_lds_dwordx4 v5, %4\n"
      "s_waitcnt lgkmcnt(0)\n"
      "s_barrier\n"
      "s_nop 0\n"
      "v_pk_fma_f32 v[232:233], v[232:233], s[54:55], s[54:55]\n"
      "v_pk_fma_f32 v[234:235], v[234:235], s[54:55], s[54:55]\n"
      "v_pk_fma_f32 v[236:237], v[236:237], s[54:55], s[54:55]\n"
      "v_pk_fma_f32 v[238:239], v[238:239], s[54:55], s[54:55]\n"
      "v_pk_fma_f32 v[240:241], v[240:241], s[54:55], s[54:55]\n"
      "v_pk_fma_f32 v[242:243], v[242:243], s[54:55], s[54:55]\n"
      "v_pk_fma_f32 v[244:245], v[244:245], s[54:55], s[54:55]\n"
      "v_pk_fma_f32 v[246:247], v[246:247], s[54:55], s[54:55]\n"
      "v_pk_fma_f32 v[32:33], v[200:201], v[200:201], s[52:53]\n"
      "v_pk_fma_f32 v[34:35], v[202:203], v[202:203], s[52:53]\n"
      "v_pk_fma_f32 v[36:37], v[204:205], v[204:205], s[52:53]\n"
      "v_pk_fma_f32 v[38:39], v[206:207], v[206:207], s[52:53]\n"
      "v_pk_fma_f32 v[40:41], v[208:209], v[208:209], s[52:53]\n"
      "v_pk_fma_f32 v[42:43], v[210:211], v[210:211], s[52:53]\n"
      "v_pk_fma_f32 v[44:45], v[212:213], v[212:213], s[52:53]\n"
      "v_pk_fma_f32 v[46:47], v[214:215], v[214:215], s[52:53]\n"
      "v_pk_mul_f32 v[32:33], v[32:33], v[200:201]\n"
      "v_pk_mul_f32 v[34:35], v[34:35], v[202:203]\n"
      "v_pk_mul_f32 v[36:37], v[36:37], v[204:205]\n"
      "v_pk_mul_f32 v[38:39], v[38:39], v[206:207]\n"
      "v_pk_mul_f32 v[40:41], v[40:41], v[208:209]\n"
      "v_pk_mul_f32 v[42:43], v[42:43], v[210:211]\n"
      "v_pk_mul_f32 v[44:45], v[44:45], v[212:213]\n"
      "v_pk_mul_f32 v[46:47], v[46:47], v[214:215]\n"
      "v_mfma_f32_32x32x16_bf16 v[216:231], v[136:139], v[112:115], 0\n"
      "v_exp_f32_e64 v32, v32\n"
      "v_exp_f32_e64 v33, v33\n"
      "v_exp_f32_e64 v34, v34\n"
      "v_mfma_f32_32x32x16_bf16 v[216:231], v[140:143], v[116:119], v[216:231]\n"
      "v_exp_f32_e64 v35, v35\n"
      "v_exp_f32_e64 v36, v36\n"
      "v_exp_f32_e64 v37, v37\n"
      "v_mfma_f32_32x32x16_bf16 v[216:231], v[144:147], v[120:123], v[216:231]\n"
      "v_exp_f32_e64 v38, v38\n"
      "v_exp_f32_e64 v39, v39\n"
      "v_exp_f32_e64 v40, v40\n"
      "v_mfma_f32_32x32x16_bf16 v[216:231], v[148:151], v[124:127], v[216:231]\n"
      "v_exp_f32_e64 v41, v41\n"
      "v_exp_f32_e64 v42, v42\n"
      "v_exp_f32_e64 v43, v43\n"
      "v_mfma_f32_32x32x16_bf16 v[216:231], v[152:155], v[128:131], v[216:231]\n"
      "v_exp_f32_e64 v44, v44\n"
      "v_exp_f32_e64 v45, v45\n"
      "v_exp_f32_e64 v46, v46\n"
      "v_mfma_f32_32x32x16_bf16 v[216:231], v[156:159], v[132:135], v[216:231]\n"
      "v_exp_f32_e64 v47, v47\n"
      "v_rcp_f32_e64 v232, v232\n"
      "v_rcp_f32_e64 v233, v233\n"
      "v_rcp_f32_e64 v234, v234\n"
      "v_rcp_f32_e64 v235, v235\n"
      "v_rcp_f32_e64 v236, v236\n"
      "v_rcp_f32_e64 v237, v237\n"
      "v_rcp_f32_e64 v238, v238\n"
      "v_rcp_f32_e64 v239, v239\n"
      "v_rcp_f32_e64 v240, v240\n"
      "v_rcp_f32_e64 v241, v241\n"
      "v_rcp_f32_e64 v242, v242\n"
      "v_rcp_f32_e64 v243, v243\n"
      "v_rcp_f32_e64 v244, v244\n"
      "v_rcp_f32_e64 v245, v245\n"
      "v_rcp_f32_e64 v246, v246\n"
      "v_rcp_f32_e32 v247, v247\n"
      "s_waitcnt vmcnt(0)\n"
      "ds_read_b64_tr_b16 v[136:137], v0 offset:12288\n"
      "ds_read_b64_tr_b16 v[138:139], v0 offset:13312\n"
      "ds_read_b64_tr_b16 v[140:141], v0 offset:14336\n"
      "ds_read_b64_tr_b16 v[142:143], v0 offset:15360\n"
      "ds_read_b64_tr_b16 v[144:145], v0 offset:16384\n"
      "ds_read_b64_tr_b16 v[146:147], v0 offset:17408\n"
      "ds_read_b64_tr_b16 v[148:149], v0 offset:18432\n"
      "ds_read_b64_tr_b16 v[150:151], v0 offset:19456\n"
      "ds_read_b64_tr_b16 v[152:153], v0 offset:20480\n"
      "ds_read_b64_tr_b16 v[154:155], v0 offset:21504\n"
      "ds_read_b64_tr_b16 v[156:157], v0 offset:22528\n"
      "ds_read_b64_tr_b16 v[158:159], v0 offset:23552\n"
      "ds_read_b64_tr_b16 v[160:161], v1 offset:24576\n"
      "ds_read_b64_tr_b16 v[162:163], v1 offset:26112\n"
      "ds_read_b64_tr_b16 v[164:165], v1 offset:24640\n"
      "ds_read_b64_tr_b16 v[166:167], v1 offset:26176\n"
      "ds_read_b64_tr_b16 v[168:169], v1 offset:24704\n"
      "ds_read_b64_tr_b16 v[170:171], v1 offset:26240\n"
      "ds_read_b64_tr_b16 v[172:173], v1 offset:27648\n"
      "ds_read_b64_tr_b16 v[174:175], v1 offset:29184\n"
      "ds_read_b64_tr_b16 v[176:177], v1 offset:27712\n"
      "ds_read_b64_tr_b16 v[178:179], v1 offset:29248\n"
      "ds_read_b64_tr_b16 v[180:181], v1 offset:27776\n"
      "ds_read_b64_tr_b16 v[182:183], v1 offset:29312\n"
      "s_mov_b32 m0, s67\n"
      "s_addk_i32 s67, -24576\n"
      "buffer_load_dwordx4 v7, s[44:47], s64 offen lds\n"
      "buffer_load_dwordx4 v2, s[44:47], s64 offen offset:1024 lds\n"
      "buffer_load_dwordx4 v4, s[44:47], s64 offen offset:2048 lds\n"
      "s_addk_i32 s64, 12288\n"
      "s_cmp_eq_u32 s73, 1\n"
      "s_cbranch_scc1 .Lcnx_l12\n"
      "s_lshl1_add_u32 m0, s79, 0\n"
      "s_add_u32 s97, s58, 0\n"
      "buffer_load_dwordx4 v28, s[36:39], s97 offen lds\n"
      "s_add_u32 m0, m0, 1024\n"
      "s_nop 0\n"
      "buffer_load_dwordx4 v29, s[36:39], s97 offen lds\n"
      ".Lcnx_l12:\n"
      "s_waitcnt lgkmcnt(0)\n"
      "s_barrier\n"
      "v_pk_mul_f32 v[232:233], v[232:233], v[184:185]\n"
      "v_pk_mul_f32 v[234:235], v[234:235], v[186:187]\n"
      "v_pk_mul_f32 v[236:237], v[236:237], v[188:189]\n"
      "v_pk_mul_f32 v[238:239], v[238:239], v[190:191]\n"
      "v_pk_mul_f32 v[240:241], v[240:241], v[192:193]\n"
      "v_pk_mul_f32 v[242:243], v[242:243], v[194:195]\n"
      "v_pk_mul_f32 v[244:245], v[244:245], v[196:197]\n"
      "v_pk_mul_f32 v[246:247], v[246:247], v[198:199]\n"
      "v_pk_fma_f32 v[32:33], v[32:33], s[54:55], s[54:55]\n"
      "v_pk_fma_f32 v[34:35], v[34:35], s[54:55], s[54:55]\n"
      "v_pk_fma_f32 v[36:37], v[36:37], s[54:55], s[54:55]\n"
      "v_pk_fma_f32 v[38:39], v[38:39], s[54:55], s[54:55]\n"
      "v_pk_fma_f32 v[40:41], v[40:41], s[54:55], s[54:55]\n"
      "v_pk_fma_f32 v[42:43], v[42:43], s[54:55], s[54:55]\n"
      "v_pk_fma_f32 v[44:45], v[44:45], s[54:55], s[54:55]\n"
      "v_pk_fma_f32 v[46:47], v[46:47], s[54:55], s[54:55]\n"
      "v_pk_fma_f32 v[48:49], v[216:217], v[216:217], s[52:53]\n"
      "v_pk_fma_f32 v[50:51], v[218:219], v[218:219], s[52:53]\n"
      "v_pk_fma_f32 v[52:53], v[220:221], v[220:221], s[52:53]\n"
      "v_pk_fma_f32 v[54:55], v[222:223], v[222:223], s[52:53]\n"
      "v_pk_fma_f32 v[56:57], v[224:225], v[224:225], s[52:53]\n"
      "v_pk_fma_f32 v[58:59], v[226:227], v[226:227], s[52:53]\n"
      "v_pk_fma_f32 v[60:61], v[228:229], v[228:229], s[52:53]\n"
      "v_pk_fma_f32 v[62:63], v[230:231], v[230:231], s[52:53]\n"
      "v_pk_mul_f32 v[48:49], v[48:49], v[216:217]\n"
      "v_pk_mul_f32 v[50:51], v[50:51], v[218:219]\n"
      "v_pk_mul_f32 v[52:53], v[52:53], v[220:221]\n"
      "v_pk_mul_f32 v[54:55], v[54:55], v[222:223]\n"
      "v_pk_mul_f32 v[56:57], v[56:57], v[224:225]\n"
      "v_pk_mul_f32 v[58:59], v[58:59], v[226:227]\n"
      "v_pk_mul_f32 v[60:61], v[60:61], v[228:229]\n"
      "v_pk_mul_f32 v[62:63], v[62:63], v[230:231]\n"
      "v_mfma_f32_32x32x16_bf16 v[184:199], v[136:139], v[112:115], 0\n"
      "v_cvt_pk_bf16_f32 v248, v232, v233\n"
      "v_cvt_pk_bf16_f32 v249, v234, v235\n"
      "v_exp_f32_e64 v48, v48\n"
      "v_exp_f32_e64 v49, v49\n"
      "v_mfma_f32_32x32x16_bf16 v[184:199], v[140:143], v[116:119], v[184:199]\n"
      "v_cvt_pk_bf16_f32 v250, v236, v237\n"
      "v_cvt_pk_bf16_f32 v251, v238, v239\n"
      "v_exp_f32_e64 v50, v50\n"
      "v_exp_f32_e64 v51, v51\n"
      "v_mfma_f32_32x32x16_bf16 v[184:199], v[144:147], v[120:123], v[184:199]\n"
      "v_cvt_pk_bf16_f32 v252, v240, v241\n"
      "v_cvt_pk_bf16_f32 v253, v242, v243\n"
      "v_exp_f32_e64 v52, v52\n"
      "v_exp_f32_e64 v53, v53\n"
      "v_mfma_f32_32x32x16_bf16 v[184:199], v[148:151], v[124:127], v[184:199]\n"
      "v_cvt_pk_bf16_f32 v254, v244, v245\n"
      "v_cvt_pk_bf16_f32 v255, v246, v247\n"
      "v_exp_f32_e64 v54, v54\n"
      "v_exp_f32_e64 v55, v55\n"
      "v_mfma_f32_32x32x16_bf16 v[184:199], v[152:155], v[128:131], v[184:199]\n"
      "v_exp_f32_e64 v56, v56\n"
      "v_exp_f32_e64 v57, v57\n"
      "v_exp_f32_e64 v58, v58\n"
      "v_mfma_f32_32x32x16_bf16 v[184:199], v[156:159], v[132:135], v[184:199]\n"
      "v_exp_f32_e64 v59, v59\n"
      "v_exp_f32_e64 v60, v60\n"
      "v_exp_f32_e64 v61, v61\n"
      "v_mfma_f32_32x32x16_bf16 v[64:79], v[160:163], v[248:251], 0\n"
      "v_exp_f32_e64 v62, v62\n"
      "v_exp_f32_e64 v63, v63\n"
      "v_rcp_f32_e64 v32, v32\n"
      "v_mfma_f32_32x32x16_bf16 v[64:79], v[172:175], v[252:255], v[64:79]\n"
      "v_rcp_f32_e64 v33, v33\n"
      "v_rcp_f32_e64 v34, v34\n"
      "v_rcp_f32_e64 v35, v35\n"
      "v_mfma_f32_32x32x16_bf16 v[80:95], v[164:167], v[248:251], 0\n"
      "v_rcp_f32_e64 v36, v36\n"
      "v_rcp_f32_e64 v37, v37\n"
      "v_rcp_f32_e64 v38, v38\n"
      "v_mfma_f32_32x32x16_bf16 v[80:95], v[176:179], v[252:255], v[80:95]\n"
      "v_rcp_f32_e64 v39, v39\n"
      "v_rcp_f32_e64 v40, v40\n"
      "v_rcp_f32_e64 v41, v41\n"
      "v_mfma_f32_32x32x16_bf16 v[96:111], v[168:171], v[248:251], 0\n"
      "v_rcp_f32_e64 v42, v42\n"
      "v_rcp_f32_e64 v43, v43\n"
      "v_rcp_f32_e64 v44, v44\n"
      "v_mfma_f32_32x32x16_bf16 v[96:111], v[180:183], v[252:255], v[96:111]\n"
      "v_rcp_f32_e64 v45, v45\n"
      "v_rcp_f32_e64 v46, v46\n"
      "v_rcp_f32_e32 v47, v47\n"
      "s_waitcnt vmcnt(0)\n"
      "s_cmp_eq_u32 s73, 1\n"
      "s_cbranch_scc1 .Lcnx_l13\n"
      "ds_read_b128 v[8:11], v30\n"
      "ds_read_b128 v[12:15], v31\n"
      ".Lcnx_l13:\n"
      "v_add_u32 v0, 0xffff4000, v0\n"
      "ds_read_b64_tr_b16 v[136:137], v0 offset:36864\n"
      "ds_read_b64_tr_b16 v[138:139], v0 offset:37888\n"
      "ds_read_b64_tr_b16 v[140:141], v0 offset:38912\n"
      "ds_read_b64_tr_b16 v[142:143], v0 offset:39936\n"
      "ds_read_b64_tr_b16 v[144:145], v0 offset:40960\n"
      "ds_read_b64_tr_b16 v[146:147], v0 offset:41984\n"
      "ds_read_b64_tr_b16 v[148:149], v0 offset:43008\n"
      "ds_read_b64_tr_b16 v[150:151], v0 offset:44032\n"
      "ds_read_b64_tr_b16 v[152:153], v0 offset:45056\n"
      "ds_read_b64_tr_b16 v[154:155], v0 offset:46080\n"
      "ds_read_b64_tr_b16 v[156:157], v0 offset:47104\n"
      "ds_read_b64_tr_b16 v[158:159], v0 offset:48128\n"
      "ds_read_b64_tr_b16 v[160:161], v1 offset:0\n"
      "ds_read_b64_tr_b16 v[162:163], v1 offset:1536\n"
      "ds_read_b64_tr_b16 v[164:165], v1 offset:64\n"
      "ds_read_b64_tr_b16 v[166:167], v1 offset:1600\n"
      "ds_read_b64_tr_b16 v[168:169], v1 offset:128\n"
      "ds_read_b64_tr_b16 v[170:171], v1 offset:1664\n"
      "ds_read_b64_tr_b16 v[172:173], v1 offset:3072\n"
      "ds_read_b64_tr_b16 v[174:175], v1 offset:4608\n"
      "ds_read_b64_tr_b16 v[176:177], v1 offset:3136\n"
      "ds_read_b64_tr_b16 v[178:179], v1 offset:4672\n"
      "ds_read_b64_tr_b16 v[180:181], v1 offset:3200\n"
      "ds_read_b64_tr_b16 v[182:183], v1 offset:4736\n"
      "s_waitcnt lgkmcnt(14)\n"
      "s_cmp_eq_u32 s73, 1\n"
      "s_cbranch_scc1 .Lcnx_l14\n"
      "s_lshl1_add_u32 m0, s79, 0\n"
      "s_add_u32 s97, s58, 64\n"
      "buffer_load_dwordx4 v28, s[36:39], s97 offen lds\n"
      "s_add_u32 m0, m0, 1024\n"
      "s_nop 0\n"
      "buffer_load_dwordx4 v29, s[36:39], s97 offen lds\n"
      ".Lcnx_l14:\n"
      "s_mov_b32 m0, s67\n"
      "s_addk_i32 s67, -24576\n"
      "buffer_load_dwordx4 v7, s[44:47], s64 offen lds\n"
      "buffer_load_dwordx4 v2, s[44:47], s64 offen offset:1024 lds\n"
      "buffer_load_dwordx4 v4, s[44:47], s64 offen offset:2048 lds\n"
      "s_addk_i32 s64, 12288\n"
      "s_waitcnt lgkmcnt(0)\n"
      "s_barrier\n"
      "s_nop 0\n"
      "v_pk_mul_f32 v[32:33], v[32:33], v[200:201]\n"
      "v_pk_mul_f32 v[34:35], v[34:35], v[202:203]\n"
      "v_pk_mul_f32 v[36:37], v[36:37], v[204:205]\n"
      "v_pk_mul_f32 v[38:39], v[38:39], v[206:207]\n"
      "v_pk_mul_f32 v[40:41], v[40:41], v[208:209]\n"
      "v_pk_mul_f32 v[42:43], v[42:43], v[210:211]\n"
      "v_pk_mul_f32 v[44:45], v[44:45], v[212:213]\n"
      "v_pk_mul_f32 v[46:47], v[46:47], v[214:215]\n"
      "v_pk_fma_f32 v[48:49], v[48:49], s[54:55], s[54:55]\n"
      "v_pk_fma_f32 v[50:51], v[50:51], s[54:55], s[54:55]\n"
      "v_pk_fma_f32 v[52:53], v[52:53], s[54:55], s[54:55]\n"
      "v_pk_fma_f32 v[54:55], v[54:55], s[54:55], s[54:55]\n"
      "v_pk_fma_f32 v[56:57], v[56:57], s[54:55], s[54:55]\n"
      "v_pk_fma_f32 v[58:59], v[58:59], s[54:55], s[54:55]\n"
      "v_pk_fma_f32 v[60:61], v[60:61], s[54:55], s[54:55]\n"
      "v_pk_fma_f32 v[62:63], v[62:63], s[54:55], s[54:55]\n"
      "v_pk_fma_f32 v[232:233], v[184:185], v[184:185], s[52:53]\n"
      "v_pk_fma_f32 v[234:235], v[186:187], v[186:187], s[52:53]\n"
      "v_pk_fma_f32 v[236:237], v[188:189], v[188:189], s[52:53]\n"
      "v_pk_fma_f32 v[238:239], v[190:191], v[190:191], s[52:53]\n"
      "v_pk_fma_f32 v[240:241], v[192:193], v[192:193], s[52:53]\n"
      "v_pk_fma_f32 v[242:243], v[194:195], v[194:195], s[52:53]\n"
      "v_pk_fma_f32 v[244:245], v[196:197], v[196:197], s[52:53]\n"
      "v_pk_fma_f32 v[246:247], v[198:199], v[198:199], s[52:53]\n"
      "v_pk_mul_f32 v[232:233], v[232:233], v[184:185]\n"
      "v_pk_mul_f32 v[234:235], v[234:235], v[186:187]\n"
      "v_pk_mul_f32 v[236:237], v[236:237], v[188:189]\n"
      "v_pk_mul_f32 v[238:239], v[238:239], v[190:191]\n"
      "v_pk_mul_f32 v[240:241], v[240:241], v[192:193]\n"
      "v_pk_mul_f32 v[242:243], v[242:243], v[194:195]\n"
      "v_pk_mul_f32 v[244:245], v[244:245], v[196:197]\n"
      "v_pk_mul_f32 v[246:247], v[246:247], v[198:199]\n"
      "v_mfma_f32_32x32x16_bf16 v[200:215], v[136:139], v[112:115], 0\n"
      "v_cvt_pk_bf16_f32 v248, v32, v33\n"
      "v_cvt_pk_bf16_f32 v249, v34, v35\n"
      "v_exp_f32_e64 v232, v232\n"
      "v_exp_f32_e64 v233, v233\n"
      "v_mfma_f32_32x32x16_bf16 v[200:215], v[140:143], v[116:119], v[200:215]\n"
      "v_cvt_pk_bf16_f32 v250, v36, v37\n"
      "v_cvt_pk_bf16_f32 v251, v38, v39\n"
      "v_exp_f32_e64 v234, v234\n"
      "v_exp_f32_e64 v235, v235\n"
      "v_mfma_f32_32x32x16_bf16 v[200:215], v[144:147], v[120:123], v[200:215]\n"
      "v_cvt_pk_bf16_f32 v252, v40, v41\n"
      "v_cvt_pk_bf16_f32 v253, v42, v43\n"
      "v_exp_f32_e64 v236, v236\n"
      "v_exp_f32_e64 v237, v237\n"
      "v_mfma_f32_32x32x16_bf16 v[200:215], v[148:151], v[124:127], v[200:215]\n"
      "v_cvt_pk_bf16_f32 v254, v44, v45\n"
      "v_cvt_pk_bf16_f32 v255, v46, v47\n"
      "v_exp_f32_e64 v238, v238\n"
      "v_exp_f32_e64 v239, v239\n"
      "v_mfma_f32_32x32x16_bf16 v[200:215], v[152:155], v[128:131], v[200:215]\n"
      "v_exp_f32_e64 v240, v240\n"
      "v_exp_f32_e64 v241, v241\n"
      "v_exp_f32_e64 v242, v242\n"
      "v_mfma_f32_32x32x16_bf16 v[200:215], v[156:159], v[132:135], v[200:215]\n"
      "v_exp_f32_e64 v243, v243\n"
      "v_exp_f32_e64 v244, v244\n"
      "v_exp_f32_e64 v245, v245\n"
      "v_mfma_f32_32x32x16_bf16 v[64:79], v[160:163], v[248:251], v[64:79]\n"
      "v_exp_f32_e64 v246, v246\n"
      "v_exp_f32_e64 v247, v247\n"
      "v_rcp_f32_e64 v48, v48\n"
      "v_mfma_f32_32x32x16_bf16 v[64:79], v[172:175], v[252:255], v[64:79]\n"
      "v_rcp_f32_e64 v49, v49\n"
      "v_rcp_f32_e64 v50, v50\n"
      "v_rcp_f32_e64 v51, v51\n"
      "v_mfma_f32_32x32x16_bf16 v[80:95], v[164:167], v[248:251], v[80:95]\n"
      "v_rcp_f32_e64 v52, v52\n"
      "v_rcp_f32_e64 v53, v53\n"
      "v_rcp_f32_e64 v54, v54\n"
      "v_mfma_f32_32x32x16_bf16 v[80:95], v[176:179], v[252:255], v[80:95]\n"
      "v_rcp_f32_e64 v55, v55\n"
      "v_rcp_f32_e64 v56, v56\n"
      "v_rcp_f32_e64 v57, v57\n"
      "v_mfma_f32_32x32x16_bf16 v[96:111], v[168:171], v[248:251], v[96:111]\n"
      "v_rcp_f32_e64 v58, v58\n"
      "v_rcp_f32_e64 v59, v59\n"
      "v_rcp_f32_e64 v60, v60\n"
      "v_mfma_f32_32x32x16_bf16 v[96:111], v[180:183], v[252:255], v[96:111]\n"
      "v_rcp_f32_e64 v61, v61\n"
      "v_rcp_f32_e64 v62, v62\n"
      "v_rcp_f32_e32 v63, v63\n"
      "s_waitcnt vmcnt(0)\n"
      "s_cmp_eq_u32 s73, 1\n"
      "s_cbranch_scc1 .Lcnx_l15\n"
      "ds_read_b128 v[16:19], v30\n"
      "ds_read_b128 v[20:23], v31\n"
      ".Lcnx_l15:\n"
      "ds_read_b64_tr_b16 v[136:137], v0 offset:12288\n"
      "ds_read_b64_tr_b16 v[138:139], v0 offset:13312\n"
      "ds_read_b64_tr_b16 v[140:141], v0 offset:14336\n"
      "ds_read_b64_tr_b16 v[142:143], v0 offset:15360\n"
      "ds_read_b64_tr_b16 v[144:145], v0 offset:16384\n"
      "ds_read_b64_tr_b16 v[146:147], v0 offset:17408\n"
      "ds_read_b64_tr_b16 v[148:149], v0 offset:18432\n"
      "ds_read_b64_tr_b16 v[150:151], v0 offset:19456\n"
      "ds_read_b64_tr_b16 v[152:153], v0 offset:20480\n"
      "ds_read_b64_tr_b16 v[154:155], v0 offset:21504\n"
      "ds_read_b64_tr_b16 v[156:157], v0 offset:22528\n"
      "ds_read_b64_tr_b16 v[158:159], v0 offset:23552\n"
      "v_add_u32 v1, 0xffff4000, v1\n"
      "ds_read_b64_tr_b16 v[160:161], v1 offset:24576\n"
      "ds_read_b64_tr_b16 v[162:163], v1 offset:26112\n"
      "ds_read_b64_tr_b16 v[164:165], v1 offset:24640\n"
      "ds_read_b64_tr_b16 v[166:167], v1 offset:26176\n"
      "ds_read_b64_tr_b16 v[168:169], v1 offset:24704\n"
      "ds_read_b64_tr_b16 v[170:171], v1 offset:26240\n"
      "ds_read_b64_tr_b16 v[172:173], v1 offset:27648\n"
      "ds_read_b64_tr_b16 v[174:175], v1 offset:29184\n"
      "ds_read_b64_tr_b16 v[176:177], v1 offset:27712\n"
      "ds_read_b64_tr_b16 v[178:179], v1 offset:29248\n"
      "ds_read_b64_tr_b16 v[180:181], v1 offset:27776\n"
      "ds_read_b64_tr_b16 v[182:183], v1 offset:29312\n"
      "s_waitcnt lgkmcnt(14)\n"
      "s_cmp_eq_u32 s73, 1\n"
      "s_cbranch_scc1 .Lcnx_l16\n"
      "s_lshl1_add_u32 m0, s79, 0\n"
      "s_add_u32 s97, s58, 128\n"
      "buffer_load_dwordx4 v28, s[36:39], s97 offen lds\n"
      "s_add_u32 m0, m0, 1024\n"
      "s_nop 0\n"
      "buffer_load_dwordx4 v29, s[36:39], s97 offen lds\n"
      ".Lcnx_l16:\n"
      "s_mov_b32 m0, s67\n"
      "s_addk_i32 s67, -24576\n"
      "s_nop 0\n"
      "buffer_load_dwordx4 v7, s[44:47], s64 offen lds\n"
      "buffer_load_dwordx4 v2, s[44:47], s64 offen offset:1024 lds\n"
      "buffer_load_dwordx4 v4, s[44:47], s64 offen offset:2048 lds\n"
      "s_addk_i32 s64, 12288\n"
      "s_waitcnt lgkmcnt(0)\n"
      "s_barrier\n"
      "s_nop 0\n"
      "v_pk_mul_f32 v[48:49], v[48:49], v[216:217]\n"
      "v_pk_mul_f32 v[50:51], v[50:51], v[218:219]\n"
      "v_pk_mul_f32 v[52:53], v[52:53], v[220:221]\n"
      "v_pk_mul_f32 v[54:55], v[54:55], v[222:223]\n"
      "v_pk_mul_f32 v[56:57], v[56:57], v[224:225]\n"
      "v_pk_mul_f32 v[58:59], v[58:59], v[226:227]\n"
      "v_pk_mul_f32 v[60:61], v[60:61], v[228:229]\n"
      "v_pk_mul_f32 v[62:63], v[62:63], v[230:231]\n"
      "v_pk_fma_f32 v[232:233], v[232:233], s[54:55], s[54:55]\n"
      "v_pk_fma_f32 v[234:235], v[234:235], s[54:55], s[54:55]\n"
      "v_pk_fma_f32 v[236:237], v[236:237], s[54:55], s[54:55]\n"
      "v_pk_fma_f32 v[238:239], v[238:239], s[54:55], s[54:55]\n"
      "v_pk_fma_f32 v[240:241], v[240:241], s[54:55], s[54:55]\n"
      "v_pk_fma_f32 v[242:243], v[242:243], s[54:55], s[54:55]\n"
      "v_pk_fma_f32 v[244:245], v[244:245], s[54:55], s[54:55]\n"
      "v_pk_fma_f32 v[246:247], v[246:247], s[54:55], s[54:55]\n"
      "v_pk_fma_f32 v[32:33], v[200:201], v[200:201], s[52:53]\n"
      "v_pk_fma_f32 v[34:35], v[202:203], v[202:203], s[52:53]\n"
      "v_pk_fma_f32 v[36:37], v[204:205], v[204:205], s[52:53]\n"
      "v_pk_fma_f32 v[38:39], v[206:207], v[206:207], s[52:53]\n"
      "v_pk_fma_f32 v[40:41], v[208:209], v[208:209], s[52:53]\n"
      "v_pk_fma_f32 v[42:43], v[210:211], v[210:211], s[52:53]\n"
      "v_pk_fma_f32 v[44:45], v[212:213], v[212:213], s[52:53]\n"
      "v_pk_fma_f32 v[46:47], v[214:215], v[214:215], s[52:53]\n"
      "v_pk_mul_f32 v[32:33], v[32:33], v[200:201]\n"
      "v_pk_mul_f32 v[34:35], v[34:35], v[202:203]\n"
      "v_pk_mul_f32 v[36:37], v[36:37], v[204:205]\n"
      "v_pk_mul_f32 v[38:39], v[38:39], v[206:207]\n"
      "v_pk_mul_f32 v[40:41], v[40:41], v[208:209]\n"
      "v_pk_mul_f32 v[42:43], v[42:43], v[210:211]\n"
      "v_pk_mul_f32 v[44:45], v[44:45], v[212:213]\n"
      "v_pk_mul_f32 v[46:47], v[46:47], v[214:215]\n"
      "v_mfma_f32_32x32x16_bf16 v[216:231], v[136:139], v[112:115], 0\n"
      "v_cvt_pk_bf16_f32 v248, v48, v49\n"
      "v_cvt_pk_bf16_f32 v249, v50, v51\n"
      "v_exp_f32_e64 v32, v32\n"
      "v_exp_f32_e64 v33, v33\n"
      "v_mfma_f32_32x32x16_bf16 v[216:231], v[140:143], v[116:119], v[216:231]\n"
      "v_cvt_pk_bf16_f32 v250, v52, v53\n"
      "v_cvt_pk_bf16_f32 v251, v54, v55\n"
      "v_exp_f32_e64 v34, v34\n"
      "v_exp_f32_e64 v35, v35\n"
      "v_mfma_f32_32x32x16_bf16 v[216:231], v[144:147], v[120:123], v[216:231]\n"
      "v_cvt_pk_bf16_f32 v252, v56, v57\n"
      "v_cvt_pk_bf16_f32 v253, v58, v59\n"
      "v_exp_f32_e64 v36, v36\n"
      "v_exp_f32_e64 v37, v37\n"
      "v_mfma_f32_32x32x16_bf16 v[216:231], v[148:151], v[124:127], v[216:231]\n"
      "v_cvt_pk_bf16_f32 v254, v60, v61\n"
      "v_cvt_pk_bf16_f32 v255, v62, v63\n"
      "v_exp_f32_e64 v38, v38\n"
      "v_exp_f32_e64 v39, v39\n"
      "v_mfma_f32_32x32x16_bf16 v[216:231], v[152:155], v[128:131], v[216:231]\n"
      "v_exp_f32_e64 v40, v40\n"
      "v_exp_f32_e64 v41, v41\n"
      "v_exp_f32_e64 v42, v42\n"
      "v_mfma_f32_32x32x16_bf16 v[216:231], v[156:159], v[132:135], v[216:231]\n"
      "v_exp_f32_e64 v43, v43\n"
      "v_exp_f32_e64 v44, v44\n"
      "v_exp_f32_e32 v45, v45\n"
      "s_waitcnt vmcnt(0)\n"
      "s_cmp_eq_u32 s73, 1\n"
      "s_cbranch_scc1 .Lcnx_l17\n"
      "ds_read_b128 v[24:27], v30\n"
      "ds_read_b128 v[28:31], v31\n"
      ".Lcnx_l17:\n"
      "ds_read_b128 v[136:139], v6 offset:13312\n"
      "ds_read_b128 v[140:143], v6 offset:14336\n"
      "ds_read_b128 v[144:147], v6 offset:15360\n"
      "s_mov_b32 m0, s67\n"
      "s_addk_i32 s67, -24576\n"
      "buffer_load_dwordx4 v7, s[44:47], s64 offen lds\n"
      "buffer_load_dwordx4 v2, s[44:47], s64 offen offset:1024 lds\n"
      "buffer_load_dwordx4 v4, s[44:47], s64 offen offset:2048 lds\n"
      "s_addk_i32 s64, 12288\n"
      "s_waitcnt lgkmcnt(0)\n"
      "s_barrier\n"
      "s_nop 0\n"
      "v_mfma_f32_32x32x16_bf16 v[64:79], v[160:163], v[248:251], v[64:79]\n"
      "v_exp_f32_e64 v46, v46\n"
      "v_exp_f32_e64 v47, v47\n"
      "v_rcp_f32_e64 v232, v232\n"
      "v_mfma_f32_32x32x16_bf16 v[64:79], v[172:175], v[252:255], v[64:79]\n"
      "v_rcp_f32_e64 v233, v233\n"
      "v_rcp_f32_e64 v234, v234\n"
      "v_rcp_f32_e64 v235, v235\n"
      "v_mfma_f32_32x32x16_bf16 v[80:95], v[164:167], v[248:251], v[80:95]\n"
      "v_rcp_f32_e64 v236, v236\n"
      "v_rcp_f32_e64 v237, v237\n"
      "v_rcp_f32_e64 v238, v238\n"
      "v_mfma_f32_32x32x16_bf16 v[80:95], v[176:179], v[252:255], v[80:95]\n"
      "v_rcp_f32_e64 v239, v239\n"
      "v_rcp_f32_e64 v240, v240\n"
      "v_rcp_f32_e64 v241, v241\n"
      "v_mfma_f32_32x32x16_bf16 v[96:111], v[168:171], v[248:251], v[96:111]\n"
      "v_rcp_f32_e64 v242, v242\n"
      "v_rcp_f32_e64 v243, v243\n"
      "v_rcp_f32_e64 v244, v244\n"
      "v_mfma_f32_32x32x16_bf16 v[96:111], v[180:183], v[252:255], v[96:111]\n"
      "v_rcp_f32_e64 v245, v245\n"
      "v_rcp_f32_e64 v246, v246\n"
      "v_rcp_f32_e64 v247, v247\n"
      "v_pk_mul_f32 v[232:233], v[232:233], v[184:185]\n"
      "v_pk_mul_f32 v[234:235], v[234:235], v[186:187]\n"
      "v_pk_mul_f32 v[236:237], v[236:237], v[188:189]\n"
      "v_pk_mul_f32 v[238:239], v[238:239], v[190:191]\n"
      "v_pk_mul_f32 v[240:241], v[240:241], v[192:193]\n"
      "v_pk_mul_f32 v[242:243], v[242:243], v[194:195]\n"
      "v_pk_mul_f32 v[244:245], v[244:245], v[196:197]\n"
      "v_pk_mul_f32 v[246:247], v[246:247], v[198:199]\n"
      "v_pk_fma_f32 v[32:33], v[32:33], s[54:55], s[54:55]\n"
      "v_pk_fma_f32 v[34:35], v[34:35], s[54:55], s[54:55]\n"
      "v_pk_fma_f32 v[36:37], v[36:37], s[54:55], s[54:55]\n"
      "v_pk_fma_f32 v[38:39], v[38:39], s[54:55], s[54:55]\n"
      "v_pk_fma_f32 v[40:41], v[40:41], s[54:55], s[54:55]\n"
      "v_pk_fma_f32 v[42:43], v[42:43], s[54:55], s[54:55]\n"
      "v_pk_fma_f32 v[44:45], v[44:45], s[54:55], s[54:55]\n"
      "v_pk_fma_f32 v[46:47], v[46:47], s[54:55], s[54:55]\n"
      "v_pk_fma_f32 v[48:49], v[216:217], v[216:217], s[52:53]\n"
      "v_pk_fma_f32 v[50:51], v[218:219], v[218:219], s[52:53]\n"
      "v_pk_fma_f32 v[52:53], v[220:221], v[220:221], s[52:53]\n"
      "v_pk_fma_f32 v[54:55], v[222:223], v[222:223], s[52:53]\n"
      "v_pk_fma_f32 v[56:57], v[224:225], v[224:225], s[52:53]\n"
      "v_pk_fma_f32 v[58:59], v[226:227], v[226:227], s[52:53]\n"
      "v_pk_fma_f32 v[60:61], v[228:229], v[228:229], s[52:53]\n"
      "v_pk_fma_f32 v[62:63], v[230:231], v[230:231], s[52:53]\n"
      "v_pk_mul_f32 v[48:49], v[48:49], v[216:217]\n"
      "v_pk_mul_f32 v[50:51], v[50:51], v[218:219]\n"
      "v_pk_mul_f32 v[52:53], v[52:53], v[220:221]\n"
      "v_pk_mul_f32 v[54:55], v[54:55], v[222:223]\n"
      "v_pk_mul_f32 v[56:57], v[56:57], v[224:225]\n"
      "v_pk_mul_f32 v[58:59], v[58:59], v[226:227]\n"
      "v_pk_mul_f32 v[60:61], v[60:61], v[228:229]\n"
      "v_pk_mul_f32 v[62:63], v[62:63], v[230:231]\n"
      "s_waitcnt vmcnt(0)\n"
      "s_nop 0\n"
      "ds_read_b64_tr_b16 v[160:161], v1 offset:0\n"
      "ds_read_b64_tr_b16 v[162:163], v1 offset:1536\n"
      "ds_read_b64_tr_b16 v[164:165], v1 offset:64\n"
      "ds_read_b64_tr_b16 v[166:167], v1 offset:1600\n"
      "ds_read_b64_tr_b16 v[168:169], v1 offset:128\n"
      "ds_read_b64_tr_b16 v[170:171], v1 offset:1664\n"
      "ds_read_b64_tr_b16 v[172:173], v1 offset:3072\n"
      "ds_read_b64_tr_b16 v[174:175], v1 offset:4608\n"
      "ds_read_b64_tr_b16 v[176:177], v1 offset:3136\n"
      "ds_read_b64_tr_b16 v[178:179], v1 offset:4672\n"
      "ds_read_b64_tr_b16 v[180:181], v1 offset:3200\n"
      "ds_read_b64_tr_b16 v[182:183], v1 offset:4736\n"
      "v_add_u32 v4, s87, v146\n"
      "v_add_u32 v7, s88, v146\n"
      "v_add_u32_e64 v2, s89, v146\n"
      "s_waitcnt lgkmcnt(0)\n"
      "s_barrier\n"
      "s_cmp_eq_u32 s73, 1\n"
      "s_cbranch_scc1 .Lcnx_m5o\n"
      "v_mfma_f32_32x32x16_bf16 v[64:79], v[136:139], v[8:11], v[64:79]\n"
      "v_cvt_pk_bf16_f32 v248, v232, v233\n"
      "v_cvt_pk_bf16_f32 v249, v234, v235\n"
      "v_exp_f32_e64 v48, v48\n"
      "v_exp_f32_e64 v49, v49\n"
      "v_mfma_f32_32x32x16_bf16 v[80:95], v[136:139], v[16:19], v[80:95]\n"
      "v_cvt_pk_bf16_f32 v250, v236, v237\n"
      "v_cvt_pk_bf16_f32 v251, v238, v239\n"
      "v_exp_f32_e64 v50, v50\n"
      "v_exp_f32_e64 v51, v51\n"
      "v_mfma_f32_32x32x16_bf16 v[96:111], v[136:139], v[24:27], v[96:111]\n"
      "v_cvt_pk_bf16_f32 v252, v240, v241\n"
      "v_cvt_pk_bf16_f32 v253, v242, v243\n"
      "v_exp_f32_e64 v52, v52\n"
      "v_exp_f32_e64 v53, v53\n"
      "v_mfma_f32_32x32x16_bf16 v[64:79], v[140:143], v[12:15], v[64:79]\n"
      "v_cvt_pk_bf16_f32 v254, v244, v245\n"
      "v_cvt_pk_bf16_f32 v255, v246, v247\n"
      "v_exp_f32_e64 v54, v54\n"
      "v_exp_f32_e64 v55, v55\n"
      "v_mfma_f32_32x32x16_bf16 v[80:95], v[140:143], v[20:23], v[80:95]\n"
      "v_exp_f32_e64 v56, v56\n"
      "v_exp_f32_e64 v57, v57\n"
      "v_exp_f32_e64 v58, v58\n"
      "v_mfma_f32_32x32x16_bf16 v[96:111], v[140:143], v[28:31], v[96:111]\n"
      "v_exp_f32_e64 v59, v59\n"
      "v_exp_f32_e64 v60, v60\n"
      "v_exp_f32_e64 v61, v61\n"
      "v_mfma_f32_32x32x16_bf16 v[64:79], v[160:163], v[248:251], v[64:79]\n"
      "v_exp_f32_e64 v62, v62\n"
      "v_exp_f32_e64 v63, v63\n"
      "v_rcp_f32_e64 v32, v32\n"
      "v_mfma_f32_32x32x16_bf16 v[80:95], v[164:167], v[248:251], v[80:95]\n"
      "v_rcp_f32_e64 v33, v33\n"
      "v_rcp_f32_e64 v34, v34\n"
      "v_rcp_f32_e64 v35, v35\n"
      "v_mfma_f32_32x32x16_bf16 v[96:111], v[168:171], v[248:251], v[96:111]\n"
      "v_rcp_f32_e64 v36, v36\n"
      "v_rcp_f32_e64 v37, v37\n"
      "v_rcp_f32_e64 v38, v38\n"
      "v_mfma_f32_32x32x16_bf16 v[64:79], v[172:175], v[252:255], v[64:79]\n"
      "v_rcp_f32_e64 v39, v39\n"
      "v_rcp_f32_e64 v40, v40\n"
      "v_rcp_f32_e64 v41, v41\n"
      "v_mfma_f32_32x32x16_bf16 v[80:95], v[176:179], v[252:255], v[80:95]\n"
      "v_rcp_f32_e64 v42, v42\n"
      "v_rcp_f32_e64 v43, v43\n"
      "v_rcp_f32_e64 v44, v44\n"
      "v_mfma_f32_32x32x16_bf16 v[96:111], v[180:183], v[252:255], v[96:111]\n"
      "v_rcp_f32_e64 v45, v45\n"
      "v_rcp_f32_e64 v46, v46\n"
      "v_rcp_f32_e32 v47, v47\n"
      "s_branch .Lcnx_m5e\n"
      ".Lcnx_m5o:\n"
      "v_cvt_pk_bf16_f32 v248, v232, v233\n"
      "v_cvt_pk_bf16_f32 v249, v234, v235\n"
      "v_cvt_pk_bf16_f32 v250, v236, v237\n"
      "v_cvt_pk_bf16_f32 v251, v238, v239\n"
      "s_nop 1\n"
      "s_nop 0\n"
      "v_mfma_f32_32x32x16_bf16 v[64:79], v[160:163], v[248:251], v[64:79]\n"
      "v_cvt_pk_bf16_f32 v252, v240, v241\n"
      "v_cvt_pk_bf16_f32 v253, v242, v243\n"
      "v_exp_f32_e64 v48, v48\n"
      "v_exp_f32_e64 v49, v49\n"
      "v_mfma_f32_32x32x16_bf16 v[80:95], v[164:167], v[248:251], v[80:95]\n"
      "v_cvt_pk_bf16_f32 v254, v244, v245\n"
      "v_cvt_pk_bf16_f32 v255, v246, v247\n"
      "v_exp_f32_e64 v50, v50\n"
      "v_exp_f32_e64 v51, v51\n"
      "v_mfma_f32_32x32x16_bf16 v[96:111], v[168:171], v[248:251], v[96:111]\n"
      "v_exp_f32_e64 v52, v52\n"
      "v_exp_f32_e64 v53, v53\n"
      "v_exp_f32_e64 v54, v54\n"
      "v_mfma_f32_32x32x16_bf16 v[64:79], v[172:175], v[252:255], v[64:79]\n"
      "v_exp_f32_e64 v55, v55\n"
      "v_exp_f32_e64 v56, v56\n"
      "v_exp_f32_e64 v57, v57\n"
      "v_mfma_f32_32x32x16_bf16 v[80:95], v[176:179], v[252:255], v[80:95]\n"
      "v_exp_f32_e64 v58, v58\n"
      "v_exp_f32_e64 v59, v59\n"
      "v_exp_f32_e64 v60, v60\n"
      "v_mfma_f32_32x32x16_bf16 v[96:111], v[180:183], v[252:255], v[96:111]\n"
      "v_exp_f32_e64 v61, v61\n"
      "v_exp_f32_e64 v62, v62\n"
      "v_exp_f32_e64 v63, v63\n"
      "v_rcp_f32_e64 v32, v32\n"
      "v_rcp_f32_e64 v33, v33\n"
      "v_rcp_f32_e64 v34, v34\n"
      "v_rcp_f32_e64 v35, v35\n"
      "v_rcp_f32_e64 v36, v36\n"
      "v_rcp_f32_e64 v37, v37\n"
      "v_rcp_f32_e64 v38, v38\n"
      "v_rcp_f32_e64 v39, v39\n"
      "v_rcp_f32_e64 v40, v40\n"
      "v_rcp_f32_e64 v41, v41\n"
      "v_rcp_f32_e64 v42, v42\n"
      "v_rcp_f32_e64 v43, v43\n"
      "v_rcp_f32_e64 v44, v44\n"
      "v_rcp_f32_e64 v45, v45\n"
      "v_rcp_f32_e64 v46, v46\n"
      "v_rcp_f32_e32 v47, v47\n"
      ".Lcnx_m5e:\n"
      "s_waitcnt vmcnt(0)\n"
      "v_add_u32 v1, 0xffff4000, v1\n"
      "ds_read_b64_tr_b16 v[160:161], v1 offset:24576\n"
      "ds_read_b64_tr_b16 v[162:163], v1 offset:26112\n"
      "ds_read_b64_tr_b16 v[164:165], v1 offset:24640\n"
      "ds_read_b64_tr_b16 v[166:167], v1 offset:26176\n"
      "ds_read_b64_tr_b16 v[168:169], v1 offset:24704\n"
      "ds_read_b64_tr_b16 v[170:171], v1 offset:26240\n"
      "ds_read_b64_tr_b16 v[172:173], v1 offset:27648\n"
      "ds_read_b64_tr_b16 v[174:175], v1 offset:29184\n"
      "ds_read_b64_tr_b16 v[176:177], v1 offset:27712\n"
      "ds_read_b64_tr_b16 v[178:179], v1 offset:29248\n"
      "ds_read_b64_tr_b16 v[180:181], v1 offset:27776\n"
      "ds_read_b64_tr_b16 v[182:183], v1 offset:29312\n"
      "ds_read_b64 v[148:149], v145 offset:0\n"
      "ds_read_b64 v[150:151], v145 offset:32\n"
      "ds_read_b64 v[152:153], v145 offset:64\n"
      "ds_read_b64 v[154:155], v145 offset:96\n"
      "ds_read_b64 v[156:157], v145 offset:128\n"
      "ds_read_b64 v[158:159], v145 offset:160\n"
      "ds_read_b64_tr_b16 v[112:113], v4 offset:12288\n"
      "ds_read_b64_tr_b16 v[114:115], v4 offset:14336\n"
      "ds_read_b64_tr_b16 v[120:121], v4 offset:16384\n"
      "ds_read_b64_tr_b16 v[122:123], v4 offset:18432\n"
      "ds_read_b64_tr_b16 v[128:129], v4 offset:20480\n"
      "ds_read_b64_tr_b16 v[130:131], v4 offset:22528\n"
      "ds_read_b64_tr_b16 v[8:9], v7 offset:12288\n"
      "ds_read_b64_tr_b16 v[10:11], v7 offset:14336\n"
      "ds_read_b64_tr_b16 v[16:17], v7 offset:16384\n"
      "ds_read_b64_tr_b16 v[18:19], v7 offset:18432\n"
      "ds_read_b64_tr_b16 v[24:25], v7 offset:20480\n"
      "ds_read_b64_tr_b16 v[26:27], v7 offset:22528\n"
      "ds_read_b64_tr_b16 v[184:185], v2 offset:12288\n"
      "ds_read_b64_tr_b16 v[186:187], v2 offset:14336\n"
      "ds_read_b64_tr_b16 v[192:193], v2 offset:16384\n"
      "ds_read_b64_tr_b16 v[194:195], v2 offset:18432\n"
      "ds_read_b64_tr_b16 v[136:137], v2 offset:20480\n"
      "ds_read_b64_tr_b16 v[138:139], v2 offset:22528\n"
      "s_waitcnt lgkmcnt(0)\n"
      "s_barrier\n"
      "v_pk_mul_f32 v[32:33], v[32:33], v[200:201]\n"
      "v_pk_mul_f32 v[34:35], v[34:35], v[202:203]\n"
      "v_pk_mul_f32 v[36:37], v[36:37], v[204:205]\n"
      "v_pk_mul_f32 v[38:39], v[38:39], v[206:207]\n"
      "v_pk_mul_f32 v[40:41], v[40:41], v[208:209]\n"
      "v_pk_mul_f32 v[42:43], v[42:43], v[210:211]\n"
      "v_pk_mul_f32 v[44:45], v[44:45], v[212:213]\n"
      "v_pk_mul_f32 v[46:47], v[46:47], v[214:215]\n"
      "v_pk_fma_f32 v[48:49], v[48:49], s[54:55], s[54:55]\n"
      "v_pk_fma_f32 v[50:51], v[50:51], s[54:55], s[54:55]\n"
      "v_pk_fma_f32 v[52:53], v[52:53], s[54:55], s[54:55]\n"
      "v_pk_fma_f32 v[54:55], v[54:55], s[54:55], s[54:55]\n"
      "v_pk_fma_f32 v[56:57], v[56:57], s[54:55], s[54:55]\n"
      "v_pk_fma_f32 v[58:59], v[58:59], s[54:55], s[54:55]\n"
      "v_pk_fma_f32 v[60:61], v[60:61], s[54:55], s[54:55]\n"
      "v_pk_fma_f32 v[62:63], v[62:63], s[54:55], s[54:55]\n"
      "v_cvt_pk_bf16_f32 v248, v32, v33\n"
      "v_cvt_pk_bf16_f32 v249, v34, v35\n"
      "v_cvt_pk_bf16_f32 v250, v36, v37\n"
      "v_cvt_pk_bf16_f32 v251, v38, v39\n"
      "s_nop 1\n"
      "s_nop 0\n"
      "v_mfma_f32_32x32x16_bf16 v[64:79], v[160:163], v[248:251], v[64:79]\n"
      "v_cvt_pk_bf16_f32 v252, v40, v41\n"
      "v_cvt_pk_bf16_f32 v253, v42, v43\n"
      "v_rcp_f32_e64 v48, v48\n"
      "v_rcp_f32_e64 v49, v49\n"
      "v_mfma_f32_32x32x16_bf16 v[80:95], v[164:167], v[248:251], v[80:95]\n"
      "v_cvt_pk_bf16_f32 v254, v44, v45\n"
      "v_cvt_pk_bf16_f32 v255, v46, v47\n"
      "v_rcp_f32_e64 v50, v50\n"
      "v_rcp_f32_e64 v51, v51\n"
      "v_mfma_f32_32x32x16_bf16 v[96:111], v[168:171], v[248:251], v[96:111]\n"
      "v_rcp_f32_e64 v52, v52\n"
      "v_rcp_f32_e64 v53, v53\n"
      "v_rcp_f32_e64 v54, v54\n"
      "v_mfma_f32_32x32x16_bf16 v[64:79], v[172:175], v[252:255], v[64:79]\n"
      "v_rcp_f32_e64 v55, v55\n"
      "v_rcp_f32_e64 v56, v56\n"
      "v_rcp_f32_e64 v57, v57\n"
      "v_mfma_f32_32x32x16_bf16 v[80:95], v[176:179], v[252:255], v[80:95]\n"
      "v_rcp_f32_e64 v58, v58\n"
      "v_rcp_f32_e64 v59, v59\n"
      "v_rcp_f32_e64 v60, v60\n"
      "v_mfma_f32_32x32x16_bf16 v[96:111], v[180:183], v[252:255], v[96:111]\n"
      "v_rcp_f32_e64 v61, v61\n"
      "v_rcp_f32_e64 v62, v62\n"
      "v_rcp_f32_e64 v63, v63\n"
      "s_waitcnt vmcnt(0)\n"
      "v_xor_b32 v4, 32, v4\n"
      "v_xor_b32 v7, 32, v7\n"
      "v_xor_b32 v2, 32, v2\n"
      "ds_read_b64_tr_b16 v[116:117], v4 offset:12288\n"
      "ds_read_b64_tr_b16 v[118:119], v4 offset:14336\n"
      "ds_read_b64_tr_b16 v[124:125], v4 offset:16384\n"
      "ds_read_b64_tr_b16 v[126:127], v4 offset:18432\n"
      "ds_read_b64_tr_b16 v[132:133], v4 offset:20480\n"
      "ds_read_b64_tr_b16 v[134:135], v4 offset:22528\n"
      "ds_read_b64_tr_b16 v[12:13], v7 offset:12288\n"
      "ds_read_b64_tr_b16 v[14:15], v7 offset:14336\n"
      "ds_read_b64_tr_b16 v[20:21], v7 offset:16384\n"
      "ds_read_b64_tr_b16 v[22:23], v7 offset:18432\n"
      "ds_read_b64_tr_b16 v[28:29], v7 offset:20480\n"
      "ds_read_b64_tr_b16 v[30:31], v7 offset:22528\n"
      "ds_read_b64_tr_b16 v[188:189], v2 offset:12288\n"
      "ds_read_b64_tr_b16 v[190:191], v2 offset:14336\n"
      "ds_read_b64_tr_b16 v[196:197], v2 offset:16384\n"
      "ds_read_b64_tr_b16 v[198:199], v2 offset:18432\n"
      "ds_read_b64_tr_b16 v[140:141], v2 offset:20480\n"
      "ds_read_b64_tr_b16 v[142:143], v2 offset:22528\n"
      "ds_read_b64_tr_b16 v[160:161], v1 offset:0\n"
      "ds_read_b64_tr_b16 v[162:163], v1 offset:1536\n"
      "ds_read_b64_tr_b16 v[164:165], v1 offset:64\n"
      "ds_read_b64_tr_b16 v[166:167], v1 offset:1600\n"
      "ds_read_b64_tr_b16 v[168:169], v1 offset:128\n"
      "ds_read_b64_tr_b16 v[170:171], v1 offset:1664\n"
      "ds_read_b64_tr_b16 v[172:173], v1 offset:3072\n"
      "ds_read_b64_tr_b16 v[174:175], v1 offset:4608\n"
      "ds_read_b64_tr_b16 v[176:177], v1 offset:3136\n"
      "ds_read_b64_tr_b16 v[178:179], v1 offset:4672\n"
      "ds_read_b64_tr_b16 v[180:181], v1 offset:3200\n"
      "ds_read_b64_tr_b16 v[182:183], v1 offset:4736\n"
      "s_waitcnt lgkmcnt(0)\n"
      "s_barrier\n"
      "v_mfma_f32_16x16x32_bf16 v[200:203], v[112:115], v[148:151], 0\n"
      "v_mfma_f32_16x16x32_bf16 v[204:207], v[116:119], v[148:151], 0\n"
      "v_mfma_f32_16x16x32_bf16 v[208:211], v[8:11], v[148:151], 0\n"
      "v_mfma_f32_16x16x32_bf16 v[212:215], v[12:15], v[148:151], 0\n"
      "v_mfma_f32_16x16x32_bf16 v[232:235], v[184:187], v[148:151], 0\n"
      "v_mfma_f32_16x16x32_bf16 v[236:239], v[188:191], v[148:151], 0\n"
      "v_mfma_f32_16x16x32_bf16 v[200:203], v[120:123], v[152:155], v[200:203]\n"
      "v_mfma_f32_16x16x32_bf16 v[204:207], v[124:127], v[152:155], v[204:207]\n"
      "v_mfma_f32_16x16x32_bf16 v[208:211], v[16:19], v[152:155], v[208:211]\n"
      "v_mfma_f32_16x16x32_bf16 v[212:215], v[20:23], v[152:155], v[212:215]\n"
      "v_mfma_f32_16x16x32_bf16 v[232:235], v[192:195], v[152:155], v[232:235]\n"
      "v_mfma_f32_16x16x32_bf16 v[236:239], v[196:199], v[152:155], v[236:239]\n"
      "v_mfma_f32_16x16x32_bf16 v[200:203], v[128:131], v[156:159], v[200:203]\n"
      "v_mfma_f32_16x16x32_bf16 v[204:207], v[132:135], v[156:159], v[204:207]\n"
      "v_mfma_f32_16x16x32_bf16 v[208:211], v[24:27], v[156:159], v[208:211]\n"
      "v_mfma_f32_16x16x32_bf16 v[212:215], v[28:31], v[156:159], v[212:215]\n"
      "v_mfma_f32_16x16x32_bf16 v[232:235], v[136:139], v[156:159], v[232:235]\n"
      "v_mfma_f32_16x16x32_bf16 v[236:239], v[140:143], v[156:159], v[236:239]\n"
      "s_nop 2\n"
      "s_nop 0\n"
      "v_mov_b32_dpp v200, v208 quad_perm:[0,1,2,3] row_mask:0xf bank_mask:0x2\n"
      "v_mov_b32_dpp v201, v209 quad_perm:[0,1,2,3] row_mask:0xf bank_mask:0x2\n"
      "v_mov_b32_dpp v202, v210 quad_perm:[0,1,2,3] row_mask:0xf bank_mask:0x2\n"
      "v_mov_b32_dpp v203, v211 quad_perm:[0,1,2,3] row_mask:0xf bank_mask:0x2\n"
      "v_mov_b32_dpp v204, v212 quad_perm:[0,1,2,3] row_mask:0xf bank_mask:0x2\n"
      "v_mov_b32_dpp v205, v213 quad_perm:[0,1,2,3] row_mask:0xf bank_mask:0x2\n"
      "v_mov_b32_dpp v206, v214 quad_perm:[0,1,2,3] row_mask:0xf bank_mask:0x2\n"
      "v_mov_b32_dpp v207, v215 quad_perm:[0,1,2,3] row_mask:0xf bank_mask:0x2\n"
      "v_mov_b32_dpp v200, v232 quad_perm:[0,1,2,3] row_mask:0xf bank_mask:0x4\n"
      "v_mov_b32_dpp v201, v233 quad_perm:[0,1,2,3] row_mask:0xf bank_mask:0x4\n"
      "v_mov_b32_dpp v202, v234 quad_perm:[0,1,2,3] row_mask:0xf bank_mask:0x4\n"
      "v_mov_b32_dpp v203, v235 quad_perm:[0,1,2,3] row_mask:0xf bank_mask:0x4\n"
      "v_mov_b32_dpp v204, v236 quad_perm:[0,1,2,3] row_mask:0xf bank_mask:0x4\n"
      "v_mov_b32_dpp v205, v237 quad_perm:[0,1,2,3] row_mask:0xf bank_mask:0x4\n"
      "v_mov_b32_dpp v206, v238 quad_perm:[0,1,2,3] row_mask:0xf bank_mask:0x4\n"
      "v_mov_b32_dpp v207, v239 quad_perm:[0,1,2,3] row_mask:0xf bank_mask:0x4\n"
      "s_waitcnt vmcnt(0)\n"
      "v_add_u32 v4, s90, v147\n"
      "v_add_u32 v7, s91, v147\n"
      "v_add_u32 v2, s92, v147\n"
      "ds_read_b64_tr_b16 v[112:113], v4 offset:0\n"
      "ds_read_b64_tr_b16 v[114:115], v4 offset:3072\n"
      "ds_read_b64_tr_b16 v[120:121], v4 offset:64\n"
      "ds_read_b64_tr_b16 v[122:123], v4 offset:3136\n"
      "ds_read_b64_tr_b16 v[128:129], v4 offset:128\n"
      "ds_read_b64_tr_b16 v[130:131], v4 offset:3200\n"
      "ds_read_b64_tr_b16 v[8:9], v7 offset:0\n"
      "ds_read_b64_tr_b16 v[10:11], v7 offset:3072\n"
      "ds_read_b64_tr_b16 v[16:17], v7 offset:64\n"
      "ds_read_b64_tr_b16 v[18:19], v7 offset:3136\n"
      "ds_read_b64_tr_b16 v[24:25], v7 offset:128\n"
      "ds_read_b64_tr_b16 v[26:27], v7 offset:3200\n"
      "ds_read_b64_tr_b16 v[184:185], v2 offset:0\n"
      "ds_read_b64_tr_b16 v[186:187], v2 offset:3072\n"
      "ds_read_b64_tr_b16 v[192:193], v2 offset:64\n"
      "ds_read_b64_tr_b16 v[194:195], v2 offset:3136\n"
      "ds_read_b64_tr_b16 v[136:137], v2 offset:128\n"
      "ds_read_b64_tr_b16 v[138:139], v2 offset:3200\n"
      "v_xor_b32 v4, 32, v4\n"
      "v_xor_b32 v7, 32, v7\n"
      "v_xor_b32_e64 v2, 32, v2\n"
      "ds_read_b64_tr_b16 v[116:117], v4 offset:0\n"
      "ds_read_b64_tr_b16 v[118:119], v4 offset:3072\n"
      "ds_read_b64_tr_b16 v[124:125], v4 offset:64\n"
      "ds_read_b64_tr_b16 v[126:127], v4 offset:3136\n"
      "ds_read_b64_tr_b16 v[132:133], v4 offset:128\n"
      "ds_read_b64_tr_b16 v[134:135], v4 offset:3200\n"
      "ds_read_b64_tr_b16 v[12:13], v7 offset:0\n"
      "ds_read_b64_tr_b16 v[14:15], v7 offset:3072\n"
      "ds_read_b64_tr_b16 v[20:21], v7 offset:64\n"
      "ds_read_b64_tr_b16 v[22:23], v7 offset:3136\n"
      "ds_read_b64_tr_b16 v[28:29], v7 offset:128\n"
      "ds_read_b64_tr_b16 v[30:31], v7 offset:3200\n"
      "ds_read_b64_tr_b16 v[188:189], v2 offset:0\n"
      "ds_read_b64_tr_b16 v[190:191], v2 offset:3072\n"
      "ds_read_b64_tr_b16 v[196:197], v2 offset:64\n"
      "ds_read_b64_tr_b16 v[198:199], v2 offset:3136\n"
      "ds_read_b64_tr_b16 v[140:141], v2 offset:128\n"
      "ds_read_b64_tr_b16 v[142:143], v2 offset:3200\n"
      "v_add_u32_e64 v5, s79, v6\n"
      "v_add_u32 v3, 13312, v5\n"
      "v_add_u32 v5, 7168, v5\n"
      "global_load_dwordx4 v[148:151], v5, %4\n"
      "global_load_dwordx2 v[152:153], v3, %4\n"
      "s_waitcnt lgkmcnt(0)\n"
      "s_barrier\n"
      "v_pk_mul_f32 v[48:49], v[48:49], v[216:217]\n"
      "v_pk_mul_f32 v[50:51], v[50:51], v[218:219]\n"
      "v_pk_mul_f32 v[52:53], v[52:53], v[220:221]\n"
      "v_pk_mul_f32 v[54:55], v[54:55], v[222:223]\n"
      "v_pk_mul_f32 v[56:57], v[56:57], v[224:225]\n"
      "v_pk_mul_f32 v[58:59], v[58:59], v[226:227]\n"
      "v_pk_mul_f32 v[60:61], v[60:61], v[228:229]\n"
      "v_pk_mul_f32 v[62:63], v[62:63], v[230:231]\n"
      "v_pk_fma_f32 v[32:33], v[200:201], v[200:201], s[52:53]\n"
      "v_pk_fma_f32 v[34:35], v[202:203], v[202:203], s[52:53]\n"
      "v_pk_fma_f32 v[36:37], v[204:205], v[204:205], s[52:53]\n"
      "v_pk_fma_f32 v[38:39], v[206:207], v[206:207], s[52:53]\n"
      "v_pk_mul_f32 v[32:33], v[32:33], v[200:201]\n"
      "v_pk_mul_f32 v[34:35], v[34:35], v[202:203]\n"
      "v_pk_mul_f32 v[36:37], v[36:37], v[204:205]\n"
      "v_pk_mul_f32 v[38:39], v[38:39], v[206:207]\n"
      "v_cvt_pk_bf16_f32 v248, v48, v49\n"
      "v_cvt_pk_bf16_f32 v249, v50, v51\n"
      "v_cvt_pk_bf16_f32 v250, v52, v53\n"
      "v_cvt_pk_bf16_f32 v251, v54, v55\n"
      "s_nop 1\n"
      "s_nop 0\n"
      "v_mfma_f32_32x32x16_bf16 v[64:79], v[160:163], v[248:251], v[64:79]\n"
      "v_cvt_pk_bf16_f32 v252, v56, v57\n"
      "v_cvt_pk_bf16_f32 v253, v58, v59\n"
      "v_cvt_pk_bf16_f32 v254, v60, v61\n"
      "v_cvt_pk_bf16_f32 v255, v62, v63\n"
      "v_exp_f32_e64 v32, v32\n"
      "v_mfma_f32_32x32x16_bf16 v[80:95], v[164:167], v[248:251], v[80:95]\n"
      "v_exp_f32_e64 v33, v33\n"
      "v_exp_f32_e64 v34, v34\n"
      "v_exp_f32_e64 v35, v35\n"
      "v_mfma_f32_32x32x16_bf16 v[96:111], v[168:171], v[248:251], v[96:111]\n"
      "v_exp_f32_e64 v36, v36\n"
      "v_exp_f32_e64 v37, v37\n"
      "v_exp_f32_e64 v38, v38\n"
      "v_mfma_f32_32x32x16_bf16 v[64:79], v[172:175], v[252:255], v[64:79]\n"
      "v_exp_f32_e64 v39, v39\n"
      "v_fma_f32 v32, v32, s54, s54\n"
      "v_fma_f32 v33, v33, s54, s54\n"
      "v_fma_f32 v34, v34, s54, s54\n"
      "v_fma_f32 v35, v35, s54, s54\n"
      "v_mfma_f32_32x32x16_bf16 v[80:95], v[176:179], v[252:255], v[80:95]\n"
      "v_fma_f32 v36, v36, s54, s54\n"
      "v_fma_f32 v37, v37, s54, s54\n"
      "v_fma_f32 v38, v38, s54, s54\n"
      "v_fma_f32 v39, v39, s54, s54\n"
      "v_rcp_f32_e64 v32, v32\n"
      "v_mfma_f32_32x32x16_bf16 v[96:111], v[180:183], v[252:255], v[96:111]\n"
      "v_rcp_f32_e64 v33, v33\n"
      "v_rcp_f32_e64 v34, v34\n"
      "v_rcp_f32_e64 v35, v35\n"
      "v_rcp_f32_e64 v36, v36\n"
      "v_rcp_f32_e64 v37, v37\n"
      "v_rcp_f32_e64 v38, v38\n"
      "v_rcp_f32_e64 v39, v39\n"
      "v_mul_f32_e64 v32, v32, v200\n"
      "v_mul_f32_e64 v33, v33, v201\n"
      "v_mul_f32_e64 v34, v34, v202\n"
      "v_mul_f32_e64 v35, v35, v203\n"
      "v_mul_f32_e64 v36, v36, v204\n"
      "v_mul_f32_e64 v37, v37, v205\n"
      "v_mul_f32_e64 v38, v38, v206\n"
      "v_mul_f32_e64 v39, v39, v207\n"
      "v_cvt_pk_bf16_f32 v40, v32, v33\n"
      "v_cvt_pk_bf16_f32 v41, v34, v35\n"
      "v_cvt_pk_bf16_f32 v42, v36, v37\n"
      "v_cvt_pk_bf16_f32 v43, v38, v39\n"
      "v_mov_b32_dpp v44, v40 row_shl:4 row_mask:0xf bank_mask:0xf bound_ctrl:1\n"
      "v_mov_b32_dpp v45, v41 row_shl:4 row_mask:0xf bank_mask:0xf bound_ctrl:1\n"
      "v_mov_b32_dpp v46, v42 row_shl:4 row_mask:0xf bank_mask:0xf bound_ctrl:1\n"
      "v_mov_b32_dpp v47, v43 row_shl:4 row_mask:0xf bank_mask:0xf bound_ctrl:1\n"
      "v_mov_b32_dpp v200, v40 row_shl:8 row_mask:0xf bank_mask:0xf bound_ctrl:1\n"
      "v_mov_b32_dpp v201, v41 row_shl:8 row_mask:0xf bank_mask:0xf bound_ctrl:1\n"
      "v_mov_b32_dpp v202, v42 row_shl:8 row_mask:0xf bank_mask:0xf bound_ctrl:1\n"
      "v_mov_b32_dpp v203, v43 row_shl:8 row_mask:0xf bank_mask:0xf bound_ctrl:1\n"
      "s_waitcnt vmcnt(0)\n"
      "v_add_u32 v1, s94, v6\n"
      "s_cmp_eq_u32 s73, 1\n"
      "s_cbranch_scc1 .Lcnx_l18\n"
      "ds_write_b128 v1, v[88:91] offset:6144\n"
      "ds_write_b128 v1, v[92:95] offset:7168\n"
      "ds_write_b128 v1, v[96:99] offset:8192\n"
      "ds_write_b128 v1, v[100:103] offset:9216\n"
      "ds_write_b128 v1, v[104:107] offset:10240\n"
      "ds_write_b128 v1, v[108:111] offset:11264\n"
      "s_branch .Lcnx_l19\n"
      "s_nop 0\n"
      ".Lcnx_l18:\n"
      "ds_write_b128 v1, v[64:67] offset:0\n"
      "ds_write_b128 v1, v[68:71] offset:1024\n"
      "ds_write_b128 v1, v[72:75] offset:2048\n"
      "ds_write_b128 v1, v[76:79] offset:3072\n"
      "ds_write_b128 v1, v[80:83] offset:4096\n"
      "ds_write_b128 v1, v[84:87] offset:5120\n"
      ".Lcnx_l19:\n"
      "s_and_b32 s97, s96, 3\n"
      "s_mul_i32 s98, s97, 48\n"
      "s_add_u32 s59, s58, 561024\n"
      "s_add_u32 s59, s59, s98\n"
      "v_bfe_u32 v4, v6, 4, 2\n"
      "v_mul_u32_u24 v7, 192, v4\n"
      "v_lshrrev_b32 v3, 6, v6\n"
      "v_lshl_add_u32 v7, v3, 3, v7\n"
      "s_mov_b64 exec, 0xffffff\n"
      "buffer_load_dwordx2 v[48:49], v7, s[36:39], s59 offen\n"
      "s_mov_b64 exec, -1\n"
      "v_lshrrev_b32 v3, 8, v6\n"
      "v_lshl_add_u32 v4, v3, 2, v4\n"
      "s_mul_i32 s98, s97, 1536\n"
      "s_add_u32 s98, s98, 114688\n"
      "v_lshl_add_u32 v4, v4, 4, s98\n"
      "s_mul_i32 s98, s97, 384\n"
      "s_add_u32 s98, s98, 114688\n"
      "v_add_u32 v5, s98, v6\n"
      "s_mul_i32 s98, s73, 6144\n"
      "v_add_u32 v3, s98, v1\n"
      "s_waitcnt lgkmcnt(0)\n"
      "s_barrier\n"
      "ds_read_b128 v[160:163], v3 offset:0\n"
      "ds_read_b128 v[164:167], v3 offset:1024\n"
      "ds_read_b128 v[168:171], v3 offset:2048\n"
      "ds_read_b128 v[172:175], v3 offset:3072\n"
      "ds_read_b128 v[176:179], v3 offset:4096\n"
      "ds_read_b128 v[180:183], v3 offset:5120\n"
      "v_add_u32 v0, s75, v144\n"
      "v_xor_b32 v1, 32, v0\n"
      "v_add_u32_e64 v2, s75, v6\n"
      "v_mfma_f32_16x16x32_bf16 v[216:219], v[112:115], v[40:43], 0\n"
      "v_mfma_f32_16x16x32_bf16 v[220:223], v[116:119], v[40:43], 0\n"
      "v_mfma_f32_16x16x32_bf16 v[224:227], v[120:123], v[40:43], 0\n"
      "v_mfma_f32_16x16x32_bf16 v[228:231], v[124:127], v[40:43], 0\n"
      "v_mfma_f32_16x16x32_bf16 v[232:235], v[128:131], v[40:43], 0\n"
      "v_mfma_f32_16x16x32_bf16 v[236:239], v[132:135], v[40:43], 0\n"
      "s_cmp_eq_u32 s73, 1\n"
      "s_cbranch_scc1 .Lcnx_l20\n"
      "s_waitcnt lgkmcnt(4)\n"
      "v_mfma_f32_16x16x32_bf16 v[216:219], v[8:11], v[44:47], v[216:219]\n"
      "v_add_f32 v64, v64, v160\n"
      "v_add_f32 v65, v65, v161\n"
      "v_mfma_f32_16x16x32_bf16 v[220:223], v[12:15], v[44:47], v[220:223]\n"
      "v_add_f32 v66, v66, v162\n"
      "v_add_f32 v67, v67, v163\n"
      "v_mfma_f32_16x16x32_bf16 v[224:227], v[16:19], v[44:47], v[224:227]\n"
      "v_add_f32 v68, v68, v164\n"
      "v_add_f32 v69, v69, v165\n"
      "v_mfma_f32_16x16x32_bf16 v[228:231], v[20:23], v[44:47], v[228:231]\n"
      "v_add_f32 v70, v70, v166\n"
      "v_add_f32 v71, v71, v167\n"
      "s_waitcnt lgkmcnt(2)\n"
      "v_mfma_f32_16x16x32_bf16 v[232:235], v[24:27], v[44:47], v[232:235]\n"
      "v_add_f32 v72, v72, v168\n"
      "v_add_f32 v73, v73, v169\n"
      "v_mfma_f32_16x16x32_bf16 v[236:239], v[28:31], v[44:47], v[236:239]\n"
      "v_add_f32 v74, v74, v170\n"
      "v_add_f32 v75, v75, v171\n"
      "v_mfma_f32_16x16x32_bf16 v[216:219], v[184:187], v[200:203], v[216:219]\n"
      "v_add_f32 v76, v76, v172\n"
      "v_add_f32 v77, v77, v173\n"
      "v_mfma_f32_16x16x32_bf16 v[220:223], v[188:191], v[200:203], v[220:223]\n"
      "v_add_f32 v78, v78, v174\n"
      "v_add_f32 v79, v79, v175\n"
      "s_waitcnt lgkmcnt(0)\n"
      "v_mfma_f32_16x16x32_bf16 v[224:227], v[192:195], v[200:203], v[224:227]\n"
      "v_add_f32 v80, v80, v176\n"
      "v_add_f32 v81, v81, v177\n"
      "v_mfma_f32_16x16x32_bf16 v[228:231], v[196:199], v[200:203], v[228:231]\n"
      "v_add_f32 v82, v82, v178\n"
      "v_add_f32 v83, v83, v179\n"
      "v_mfma_f32_16x16x32_bf16 v[232:235], v[136:139], v[200:203], v[232:235]\n"
      "v_add_f32 v84, v84, v180\n"
      "v_add_f32 v85, v85, v181\n"
      "v_mfma_f32_16x16x32_bf16 v[236:239], v[140:143], v[200:203], v[236:239]\n"
      "v_add_f32 v86, v86, v182\n"
      "v_add_f32_e64 v87, v87, v183\n"
      "v_cvt_pk_bf16_f32 v8, v64, v65\n"
      "v_cvt_pk_bf16_f32 v9, v66, v67\n"
      "v_cvt_pk_bf16_f32 v10, v68, v69\n"
      "v_cvt_pk_bf16_f32 v11, v70, v71\n"
      "ds_write_b128 v0, v[8:11] offset:0\n"
      "v_cvt_pk_bf16_f32 v12, v72, v73\n"
      "v_cvt_pk_bf16_f32 v13, v74, v75\n"
      "v_cvt_pk_bf16_f32 v14, v76, v77\n"
      "v_cvt_pk_bf16_f32 v15, v78, v79\n"
      "ds_write_b128 v1, v[12:15] offset:0\n"
      "v_cvt_pk_bf16_f32 v16, v80, v81\n"
      "v_cvt_pk_bf16_f32 v17, v82, v83\n"
      "v_cvt_pk_bf16_f32 v18, v84, v85\n"
      "v_cvt_pk_bf16_f32 v19, v86, v87\n"
      "ds_write_b128 v0, v[16:19] offset:64\n"
      "s_branch .Lcnx_l21\n"
      ".Lcnx_l20:\n"
      "s_waitcnt lgkmcnt(4)\n"
      "v_mfma_f32_16x16x32_bf16 v[216:219], v[8:11], v[44:47], v[216:219]\n"
      "v_add_f32 v88, v88, v160\n"
      "v_add_f32 v89, v89, v161\n"
      "v_mfma_f32_16x16x32_bf16 v[220:223], v[12:15], v[44:47], v[220:223]\n"
      "v_add_f32 v90, v90, v162\n"
      "v_add_f32 v91, v91, v163\n"
      "v_mfma_f32_16x16x32_bf16 v[224:227], v[16:19], v[44:47], v[224:227]\n"
      "v_add_f32 v92, v92, v164\n"
      "v_add_f32 v93, v93, v165\n"
      "v_mfma_f32_16x16x32_bf16 v[228:231], v[20:23], v[44:47], v[228:231]\n"
      "v_add_f32 v94, v94, v166\n"
      "v_add_f32 v95, v95, v167\n"
      "s_waitcnt lgkmcnt(2)\n"
      "v_mfma_f32_16x16x32_bf16 v[232:235], v[24:27], v[44:47], v[232:235]\n"
      "v_add_f32 v96, v96, v168\n"
      "v_add_f32 v97, v97, v169\n"
      "v_mfma_f32_16x16x32_bf16 v[236:239], v[28:31], v[44:47], v[236:239]\n"
      "v_add_f32 v98, v98, v170\n"
      "v_add_f32 v99, v99, v171\n"
      "v_mfma_f32_16x16x32_bf16 v[216:219], v[184:187], v[200:203], v[216:219]\n"
      "v_add_f32 v100, v100, v172\n"
      "v_add_f32 v101, v101, v173\n"
      "v_mfma_f32_16x16x32_bf16 v[220:223], v[188:191], v[200:203], v[220:223]\n"
      "v_add_f32 v102, v102, v174\n"
      "v_add_f32 v103, v103, v175\n"
      "s_waitcnt lgkmcnt(0)\n"
      "v_mfma_f32_16x16x32_bf16 v[224:227], v[192:195], v[200:203], v[224:227]\n"
      "v_add_f32 v104, v104, v176\n"
      "v_add_f32 v105, v105, v177\n"
      "v_mfma_f32_16x16x32_bf16 v[228:231], v[196:199], v[200:203], v[228:231]\n"
      "v_add_f32 v106, v106, v178\n"
      "v_add_f32 v107, v107, v179\n"
      "v_mfma_f32_16x16x32_bf16 v[232:235], v[136:139], v[200:203], v[232:235]\n"
      "v_add_f32 v108, v108, v180\n"
      "v_add_f32 v109, v109, v181\n"
      "v_mfma_f32_16x16x32_bf16 v[236:239], v[140:143], v[200:203], v[236:239]\n"
      "v_add_f32 v110, v110, v182\n"
      "v_add_f32 v111, v111, v183\n"
      "v_cvt_pk_bf16_f32 v20, v88, v89\n"
      "v_cvt_pk_bf16_f32 v21, v90, v91\n"
      "v_cvt_pk_bf16_f32 v22, v92, v93\n"
      "v_cvt_pk_bf16_f32 v23, v94, v95\n"
      "ds_write_b128 v1, v[20:23] offset:64\n"
      "v_cvt_pk_bf16_f32 v24, v96, v97\n"
      "v_cvt_pk_bf16_f32 v25, v98, v99\n"
      "v_cvt_pk_bf16_f32 v26, v100, v101\n"
      "v_cvt_pk_bf16_f32 v27, v102, v103\n"
      "ds_write_b128 v0, v[24:27] offset:128\n"
      "v_cvt_pk_bf16_f32 v28, v104, v105\n"
      "v_cvt_pk_bf16_f32 v29, v106, v107\n"
      "v_cvt_pk_bf16_f32 v30, v108, v109\n"
      "v_cvt_pk_bf16_f32 v31, v110, v111\n"
      "ds_write_b128 v1, v[28:31] offset:128\n"
      ".Lcnx_l21:\n"
      "s_mov_b32 exec_lo, 0x000f000f\n"
      "s_mov_b32 exec_hi, 0x000f000f\n"
      "ds_write_b128 v4, v[216:219] offset:0\n"
      "ds_write_b128 v4, v[220:223] offset:256\n"
      "ds_write_b128 v4, v[224:227] offset:512\n"
      "ds_write_b128 v4, v[228:231] offset:768\n"
      "ds_write_b128 v4, v[232:235] offset:1024\n"
      "ds_write_b128 v4, v[236:239] offset:1280\n"
      "s_mov_b64 exec, -1\n"
      "s_mul_i32 s98, s73, 3072\n"
      "v_add_u32 v2, s98, v2\n"
      "s_waitcnt vmcnt(0) lgkmcnt(0)\n"
      "s_barrier\n"
      "ds_read_b128 v[184:187], v2 offset:0\n"
      "ds_read_b128 v[188:191], v2 offset:1024\n"
      "ds_read_b128 v[192:195], v2 offset:2048\n"
      "ds_read_b128 v[64:67], v5 offset:0\n"
      "ds_read_b128 v[68:71], v5 offset:1536\n"
      "ds_read_b128 v[72:75], v5 offset:3072\n"
      "ds_read_b128 v[76:79], v5 offset:4608\n"
      "v_lshlrev_b32 v80, 16, v48\n"
      "v_and_b32 v81, 0xffff0000, v48\n"
      "v_lshlrev_b32 v82, 16, v49\n"
      "v_and_b32 v83, 0xffff0000, v49\n"
      "s_waitcnt lgkmcnt(4)\n"
      "s_cmp_eq_u32 s73, 1\n"
      "s_cbranch_scc1 .Lcnx_l22\n"
      "s_nop 0\n"
      "buffer_store_dwordx4 v[184:187], v148, s[48:51], s58 offen nt\n"
      "buffer_store_dwordx4 v[188:191], v149, s[48:51], s58 offen nt\n"
      "buffer_store_dwordx4 v[192:195], v150, s[48:51], s58 offen nt\n"
      "s_branch .Lcnx_l23\n"
      "s_nop 0\n"
      ".Lcnx_l22:\n"
      "buffer_store_dwordx4 v[184:187], v151, s[48:51], s58 offen nt\n"
      "buffer_store_dwordx4 v[188:191], v152, s[48:51], s58 offen nt\n"
      "buffer_store_dwordx4 v[192:195], v153, s[48:51], s58 offen nt\n"
      ".Lcnx_l23:\n"
      "s_waitcnt lgkmcnt(0)\n"
      "v_add_f32 v80, v80, v64\n"
      "v_add_f32 v81, v81, v65\n"
      "v_add_f32 v82, v82, v66\n"
      "v_add_f32 v83, v83, v67\n"
      "v_add_f32 v80, v80, v68\n"
      "v_add_f32 v81, v81, v69\n"
      "v_add_f32 v82, v82, v70\n"
      "v_add_f32 v83, v83, v71\n"
      "v_add_f32 v80, v80, v72\n"
      "v_add_f32 v81, v81, v73\n"
      "v_add_f32 v82, v82, v74\n"
      "v_add_f32 v83, v83, v75\n"
      "v_add_f32 v80, v80, v76\n"
      "v_add_f32 v81, v81, v77\n"
      "v_add_f32 v82, v82, v78\n"
      "v_add_f32_e64 v83, v83, v79\n"
      "v_cvt_pk_bf16_f32 v48, v80, v81\n"
      "v_cvt_pk_bf16_f32 v49, v82, v83\n"
      "s_mov_b64 exec, 0xffffff\n"
      "buffer_store_dwordx2 v[48:49], v7, s[48:51], s59 offen nt\n"
      "s_mov_b64 exec, -1\n"
      "s_branch .Lcnx_done\n"
      ".Lcnx_astream:\n"
      "ds_read_b64 v[112:113], v2 offset:0\n"
      "ds_read_b64 v[114:115], v2 offset:16\n"
      "ds_read_b64 v[116:117], v2 offset:32\n"
      "ds_read_b64 v[118:119], v2 offset:48\n"
      "ds_read_b64 v[120:121], v2 offset:64\n"
      "ds_read_b64 v[122:123], v2 offset:80\n"
      "ds_read_b64 v[124:125], v2 offset:96\n"
      "ds_read_b64 v[126:127], v2 offset:112\n"
      "ds_read_b64 v[128:129], v2 offset:128\n"
      "ds_read_b64 v[130:131], v2 offset:144\n"
      "ds_read_b64 v[132:133], v2 offset:160\n"
      "ds_read_b64 v[134:135], v2 offset:176\n"
      "s_waitcnt lgkmcnt(10)\n"
      "s_nop 0\n"
      "v_mfma_f32_32x32x16_bf16 v[184:199], v[160:163], v[112:115], 0\n"
      "ds_read_b64_tr_b16 v[136:137], v4 offset:12288\n"
      "ds_read_b64_tr_b16 v[138:139], v4 offset:13312\n"
      "s_waitcnt lgkmcnt(10)\n"
      "s_nop 0\n"
      "v_mfma_f32_32x32x16_bf16 v[184:199], v[164:167], v[116:119], v[184:199]\n"
      "ds_read_b64_tr_b16 v[140:141], v4 offset:14336\n"
      "ds_read_b64_tr_b16 v[142:143], v4 offset:15360\n"
      "s_waitcnt lgkmcnt(10)\n"
      "s_nop 0\n"
      "v_mfma_f32_32x32x16_bf16 v[184:199], v[168:171], v[120:123], v[184:199]\n"
      "ds_read_b64_tr_b16 v[144:145], v4 offset:16384\n"
      "ds_read_b64_tr_b16 v[146:147], v4 offset:17408\n"
      "s_waitcnt lgkmcnt(10)\n"
      "s_nop 0\n"
      "v_mfma_f32_32x32x16_bf16 v[184:199], v[172:175], v[124:127], v[184:199]\n"
      "ds_read_b64_tr_b16 v[148:149], v4 offset:18432\n"
      "ds_read_b64_tr_b16 v[150:151], v4 offset:19456\n"
      "s_waitcnt lgkmcnt(10)\n"
      "s_nop 0\n"
      "v_mfma_f32_32x32x16_bf16 v[184:199], v[176:179], v[128:131], v[184:199]\n"
      "ds_read_b64_tr_b16 v[152:153], v4 offset:20480\n"
      "ds_read_b64_tr_b16 v[154:155], v4 offset:21504\n"
      "s_waitcnt lgkmcnt(10)\n"
      "s_nop 0\n"
      "v_mfma_f32_32x32x16_bf16 v[184:199], v[180:183], v[132:135], v[184:199]\n"
      "ds_read_b64_tr_b16 v[156:157], v4 offset:22528\n"
      "ds_read_b64_tr_b16 v[158:159], v4 offset:23552\n"
      "s_nop 9\n"
      "s_nop 0\n"
      "v_pk_fma_f32 v[232:233], v[184:185], v[184:185], s[52:53]\n"
      "v_pk_fma_f32 v[234:235], v[186:187], v[186:187], s[52:53]\n"
      "v_pk_fma_f32 v[236:237], v[188:189], v[188:189], s[52:53]\n"
      "v_pk_fma_f32 v[238:239], v[190:191], v[190:191], s[52:53]\n"
      "v_pk_fma_f32 v[240:241], v[192:193], v[192:193], s[52:53]\n"
      "v_pk_fma_f32 v[242:243], v[194:195], v[194:195], s[52:53]\n"
      "v_pk_fma_f32 v[244:245], v[196:197], v[196:197], s[52:53]\n"
      "v_pk_fma_f32 v[246:247], v[198:199], v[198:199], s[52:53]\n"
      "v_pk_mul_f32 v[232:233], v[232:233], v[184:185]\n"
      "v_pk_mul_f32 v[234:235], v[234:235], v[186:187]\n"
      "v_pk_mul_f32 v[236:237], v[236:237], v[188:189]\n"
      "v_pk_mul_f32 v[238:239], v[238:239], v[190:191]\n"
      "v_pk_mul_f32 v[240:241], v[240:241], v[192:193]\n"
      "v_pk_mul_f32 v[242:243], v[242:243], v[194:195]\n"
      "v_pk_mul_f32 v[244:245], v[244:245], v[196:197]\n"
      "v_pk_mul_f32 v[246:247], v[246:247], v[198:199]\n"
      "s_waitcnt lgkmcnt(0)\n"
      "s_nop 0\n"
      "v_mfma_f32_32x32x16_bf16 v[200:215], v[136:139], v[112:115], 0\n"
      "v_exp_f32_e64 v232, v232\n"
      "v_exp_f32_e64 v233, v233\n"
      "v_exp_f32_e64 v234, v234\n"
      "v_mfma_f32_32x32x16_bf16 v[200:215], v[140:143], v[116:119], v[200:215]\n"
      "v_exp_f32_e64 v235, v235\n"
      "v_exp_f32_e64 v236, v236\n"
      "v_exp_f32_e64 v237, v237\n"
      "v_mfma_f32_32x32x16_bf16 v[200:215], v[144:147], v[120:123], v[200:215]\n"
      "v_exp_f32_e64 v238, v238\n"
      "v_exp_f32_e64 v239, v239\n"
      "v_exp_f32_e64 v240, v240\n"
      "v_mfma_f32_32x32x16_bf16 v[200:215], v[148:151], v[124:127], v[200:215]\n"
      "v_exp_f32_e64 v241, v241\n"
      "v_exp_f32_e64 v242, v242\n"
      "v_exp_f32_e64 v243, v243\n"
      "v_mfma_f32_32x32x16_bf16 v[200:215], v[152:155], v[128:131], v[200:215]\n"
      "s_waitcnt vmcnt(3)\n"
      "s_setprio 3\n"
      "s_barrier\n"
      "s_nop 0\n"
      "v_exp_f32_e64 v244, v244\n"
      "v_exp_f32_e64 v245, v245\n"
      "v_exp_f32_e64 v246, v246\n"
      "v_mfma_f32_32x32x16_bf16 v[200:215], v[156:159], v[132:135], v[200:215]\n"
      "v_exp_f32_e32 v247, v247\n"
      "s_setprio 0\n"
      "ds_read_b64_tr_b16 v[136:137], v0 offset:12288\n"
      "ds_read_b64_tr_b16 v[138:139], v0 offset:13312\n"
      "ds_read_b64_tr_b16 v[140:141], v0 offset:14336\n"
      "ds_read_b64_tr_b16 v[142:143], v0 offset:15360\n"
      "ds_read_b64_tr_b16 v[144:145], v0 offset:16384\n"
      "ds_read_b64_tr_b16 v[146:147], v0 offset:17408\n"
      "ds_read_b64_tr_b16 v[148:149], v0 offset:18432\n"
      "ds_read_b64_tr_b16 v[150:151], v0 offset:19456\n"
      "ds_read_b64_tr_b16 v[152:153], v0 offset:20480\n"
      "ds_read_b64_tr_b16 v[154:155], v0 offset:21504\n"
      "ds_read_b64_tr_b16 v[156:157], v0 offset:22528\n"
      "ds_read_b64_tr_b16 v[158:159], v0 offset:23552\n"
      "s_mov_b32 m0, s67\n"
      "s_nop 0\n"
      "s_add_u32 s98, s64, 5120\n"
      "buffer_load_dwordx4 v7, s[44:47], s64 offen lds\n"
      "buffer_load_dwordx4 v7, s[44:47], s98 offen offset:1024 lds\n"
      "s_addk_i32 s98, 5120\n"
      "s_addk_i32 s64, 128\n"
      "buffer_load_dwordx4 v7, s[44:47], s98 offen offset:2048 lds\n"
      "s_addk_i32 s67, -24576\n"
      "s_nop 0\n"
      "v_pk_fma_f32 v[232:233], v[232:233], s[54:55], s[54:55]\n"
      "v_pk_fma_f32 v[234:235], v[234:235], s[54:55], s[54:55]\n"
      "v_pk_fma_f32 v[236:237], v[236:237], s[54:55], s[54:55]\n"
      "v_pk_fma_f32 v[238:239], v[238:239], s[54:55], s[54:55]\n"
      "v_pk_fma_f32 v[240:241], v[240:241], s[54:55], s[54:55]\n"
      "v_pk_fma_f32 v[242:243], v[242:243], s[54:55], s[54:55]\n"
      "v_pk_fma_f32 v[244:245], v[244:245], s[54:55], s[54:55]\n"
      "v_pk_fma_f32 v[246:247], v[246:247], s[54:55], s[54:55]\n"
      "v_pk_fma_f32 v[32:33], v[200:201], v[200:201], s[52:53]\n"
      "v_pk_fma_f32 v[34:35], v[202:203], v[202:203], s[52:53]\n"
      "v_pk_fma_f32 v[36:37], v[204:205], v[204:205], s[52:53]\n"
      "v_pk_fma_f32 v[38:39], v[206:207], v[206:207], s[52:53]\n"
      "v_pk_fma_f32 v[40:41], v[208:209], v[208:209], s[52:53]\n"
      "v_pk_fma_f32 v[42:43], v[210:211], v[210:211], s[52:53]\n"
      "v_pk_fma_f32 v[44:45], v[212:213], v[212:213], s[52:53]\n"
      "v_pk_fma_f32 v[46:47], v[214:215], v[214:215], s[52:53]\n"
      "v_pk_mul_f32 v[32:33], v[32:33], v[200:201]\n"
      "v_pk_mul_f32 v[34:35], v[34:35], v[202:203]\n"
      "v_pk_mul_f32 v[36:37], v[36:37], v[204:205]\n"
      "v_pk_mul_f32 v[38:39], v[38:39], v[206:207]\n"
      "v_pk_mul_f32 v[40:41], v[40:41], v[208:209]\n"
      "v_pk_mul_f32 v[42:43], v[42:43], v[210:211]\n"
      "v_pk_mul_f32 v[44:45], v[44:45], v[212:213]\n"
      "v_pk_mul_f32 v[46:47], v[46:47], v[214:215]\n"
      "s_waitcnt lgkmcnt(0)\n"
      "s_nop 0\n"
      "v_mfma_f32_32x32x16_bf16 v[216:231], v[136:139], v[112:115], 0\n"
      "v_exp_f32_e64 v32, v32\n"
      "v_exp_f32_e64 v33, v33\n"
      "v_exp_f32_e64 v34, v34\n"
      "v_mfma_f32_32x32x16_bf16 v[216:231], v[140:143], v[116:119], v[216:231]\n"
      "v_exp_f32_e64 v35, v35\n"
      "v_exp_f32_e64 v36, v36\n"
      "v_exp_f32_e64 v37, v37\n"
      "v_mfma_f32_32x32x16_bf16 v[216:231], v[144:147], v[120:123], v[216:231]\n"
      "v_exp_f32_e64 v38, v38\n"
      "v_exp_f32_e64 v39, v39\n"
      "v_exp_f32_e64 v40, v40\n"
      "v_mfma_f32_32x32x16_bf16 v[216:231], v[148:151], v[124:127], v[216:231]\n"
      "v_exp_f32_e64 v41, v41\n"
      "v_exp_f32_e64 v42, v42\n"
      "v_exp_f32_e64 v43, v43\n"
      "v_mfma_f32_32x32x16_bf16 v[216:231], v[152:155], v[128:131], v[216:231]\n"
      "v_exp_f32_e64 v44, v44\n"
      "v_exp_f32_e64 v45, v45\n"
      "v_exp_f32_e64 v46, v46\n"
      "v_mfma_f32_32x32x16_bf16 v[216:231], v[156:159], v[132:135], v[216:231]\n"
      "v_exp_f32_e64 v47, v47\n"
      "v_rcp_f32_e64 v232, v232\n"
      "v_rcp_f32_e64 v233, v233\n"
      "v_rcp_f32_e64 v234, v234\n"
      "v_rcp_f32_e64 v235, v235\n"
      "v_rcp_f32_e64 v236, v236\n"
      "v_rcp_f32_e64 v237, v237\n"
      "v_rcp_f32_e64 v238, v238\n"
      "v_rcp_f32_e32 v239, v239\n"
      "s_waitcnt vmcnt(3)\n"
      "s_setprio 3\n"
      "s_barrier\n"
      "v_rcp_f32_e64 v240, v240\n"
      "v_rcp_f32_e64 v241, v241\n"
      "v_rcp_f32_e64 v242, v242\n"
      "v_rcp_f32_e64 v243, v243\n"
      "v_rcp_f32_e64 v244, v244\n"
      "v_rcp_f32_e64 v245, v245\n"
      "v_rcp_f32_e64 v246, v246\n"
      "v_rcp_f32_e32 v247, v247\n"
      "s_setprio 0\n"
      "ds_read_b64_tr_b16 v[136:137], v0 offset:12800\n"
      "ds_read_b64_tr_b16 v[138:139], v0 offset:13824\n"
      "ds_read_b64_tr_b16 v[140:141], v0 offset:14848\n"
      "ds_read_b64_tr_b16 v[142:143], v0 offset:15872\n"
      "ds_read_b64_tr_b16 v[144:145], v0 offset:16896\n"
      "ds_read_b64_tr_b16 v[146:147], v0 offset:17920\n"
      "ds_read_b64_tr_b16 v[148:149], v0 offset:18944\n"
      "ds_read_b64_tr_b16 v[150:151], v0 offset:19968\n"
      "ds_read_b64_tr_b16 v[152:153], v0 offset:20992\n"
      "ds_read_b64_tr_b16 v[154:155], v0 offset:22016\n"
      "ds_read_b64_tr_b16 v[156:157], v0 offset:23040\n"
      "ds_read_b64_tr_b16 v[158:159], v0 offset:24064\n"
      "ds_read_b64_tr_b16 v[160:161], v1 offset:24576\n"
      "ds_read_b64_tr_b16 v[162:163], v1 offset:26112\n"
      "ds_read_b64_tr_b16 v[164:165], v1 offset:24640\n"
      "ds_read_b64_tr_b16 v[166:167], v1 offset:26176\n"
      "ds_read_b64_tr_b16 v[168:169], v1 offset:24704\n"
      "ds_read_b64_tr_b16 v[170:171], v1 offset:26240\n"
      "ds_read_b64_tr_b16 v[172:173], v1 offset:27648\n"
      "ds_read_b64_tr_b16 v[174:175], v1 offset:29184\n"
      "ds_read_b64_tr_b16 v[176:177], v1 offset:27712\n"
      "ds_read_b64_tr_b16 v[178:179], v1 offset:29248\n"
      "ds_read_b64_tr_b16 v[180:181], v1 offset:27776\n"
      "ds_read_b64_tr_b16 v[182:183], v1 offset:29312\n"
      "s_mov_b32 m0, s67\n"
      "s_nop 0\n"
      "s_add_u32 s98, s64, 5120\n"
      "buffer_load_dwordx4 v7, s[44:47], s64 offen lds\n"
      "buffer_load_dwordx4 v7, s[44:47], s98 offen offset:1024 lds\n"
      "s_addk_i32 s98, 5120\n"
      "s_addk_i32 s64, 128\n"
      "buffer_load_dwordx4 v7, s[44:47], s98 offen offset:2048 lds\n"
      "s_addk_i32 s67, -24576\n"
      "s_nop 0\n"
      "v_pk_mul_f32 v[232:233], v[232:233], v[184:185]\n"
      "v_pk_mul_f32 v[234:235], v[234:235], v[186:187]\n"
      "v_pk_mul_f32 v[236:237], v[236:237], v[188:189]\n"
      "v_pk_mul_f32 v[238:239], v[238:239], v[190:191]\n"
      "v_pk_mul_f32 v[240:241], v[240:241], v[192:193]\n"
      "v_pk_mul_f32 v[242:243], v[242:243], v[194:195]\n"
      "v_pk_mul_f32 v[244:245], v[244:245], v[196:197]\n"
      "v_pk_mul_f32 v[246:247], v[246:247], v[198:199]\n"
      "v_pk_fma_f32 v[32:33], v[32:33], s[54:55], s[54:55]\n"
      "v_pk_fma_f32 v[34:35], v[34:35], s[54:55], s[54:55]\n"
      "v_pk_fma_f32 v[36:37], v[36:37], s[54:55], s[54:55]\n"
      "v_pk_fma_f32 v[38:39], v[38:39], s[54:55], s[54:55]\n"
      "v_pk_fma_f32 v[40:41], v[40:41], s[54:55], s[54:55]\n"
      "v_pk_fma_f32 v[42:43], v[42:43], s[54:55], s[54:55]\n"
      "v_pk_fma_f32 v[44:45], v[44:45], s[54:55], s[54:55]\n"
      "v_pk_fma_f32 v[46:47], v[46:47], s[54:55], s[54:55]\n"
      "v_pk_fma_f32 v[48:49], v[216:217], v[216:217], s[52:53]\n"
      "v_pk_fma_f32 v[50:51], v[218:219], v[218:219], s[52:53]\n"
      "v_pk_fma_f32 v[52:53], v[220:221], v[220:221], s[52:53]\n"
      "v_pk_fma_f32 v[54:55], v[222:223], v[222:223], s[52:53]\n"
      "v_pk_fma_f32 v[56:57], v[224:225], v[224:225], s[52:53]\n"
      "v_pk_fma_f32 v[58:59], v[226:227], v[226:227], s[52:53]\n"
      "v_pk_fma_f32 v[60:61], v[228:229], v[228:229], s[52:53]\n"
      "v_pk_fma_f32 v[62:63], v[230:231], v[230:231], s[52:53]\n"
      "v_pk_mul_f32 v[48:49], v[48:49], v[216:217]\n"
      "v_pk_mul_f32 v[50:51], v[50:51], v[218:219]\n"
      "v_pk_mul_f32 v[52:53], v[52:53], v[220:221]\n"
      "v_pk_mul_f32 v[54:55], v[54:55], v[222:223]\n"
      "v_pk_mul_f32 v[56:57], v[56:57], v[224:225]\n"
      "v_pk_mul_f32 v[58:59], v[58:59], v[226:227]\n"
      "v_pk_mul_f32 v[60:61], v[60:61], v[228:229]\n"
      "v_pk_mul_f32 v[62:63], v[62:63], v[230:231]\n"
      "v_add_u32 v0, 0xffff4000, v0\n"
      "s_waitcnt lgkmcnt(12)\n"
      "s_nop 0\n"
      "v_mfma_f32_32x32x16_bf16 v[184:199], v[136:139], v[112:115], 0\n"
      "v_cvt_pk_bf16_f32 v248, v232, v233\n"
      "v_cvt_pk_bf16_f32 v249, v234, v235\n"
      "v_exp_f32_e64 v48, v48\n"
      "v_exp_f32_e64 v49, v49\n"
      "v_mfma_f32_32x32x16_bf16 v[184:199], v[140:143], v[116:119], v[184:199]\n"
      "v_cvt_pk_bf16_f32 v250, v236, v237\n"
      "v_cvt_pk_bf16_f32 v251, v238, v239\n"
      "v_exp_f32_e64 v50, v50\n"
      "v_exp_f32_e64 v51, v51\n"
      "v_mfma_f32_32x32x16_bf16 v[184:199], v[144:147], v[120:123], v[184:199]\n"
      "v_cvt_pk_bf16_f32 v252, v240, v241\n"
      "v_cvt_pk_bf16_f32 v253, v242, v243\n"
      "v_exp_f32_e64 v52, v52\n"
      "v_exp_f32_e64 v53, v53\n"
      "v_mfma_f32_32x32x16_bf16 v[184:199], v[148:151], v[124:127], v[184:199]\n"
      "v_cvt_pk_bf16_f32 v254, v244, v245\n"
      "v_cvt_pk_bf16_f32 v255, v246, v247\n"
      "v_exp_f32_e64 v54, v54\n"
      "v_exp_f32_e64 v55, v55\n"
      "v_mfma_f32_32x32x16_bf16 v[184:199], v[152:155], v[128:131], v[184:199]\n"
      "v_exp_f32_e64 v56, v56\n"
      "v_exp_f32_e64 v57, v57\n"
      "v_exp_f32_e64 v58, v58\n"
      "v_mfma_f32_32x32x16_bf16 v[184:199], v[156:159], v[132:135], v[184:199]\n"
      "v_exp_f32_e64 v59, v59\n"
      "v_exp_f32_e64 v60, v60\n"
      "v_exp_f32_e32 v61, v61\n"
      "s_waitcnt lgkmcnt(0)\n"
      "v_mfma_f32_32x32x16_bf16 v[64:79], v[160:163], v[248:251], 0\n"
      "v_exp_f32_e64 v62, v62\n"
      "v_exp_f32_e64 v63, v63\n"
      "v_rcp_f32_e64 v32, v32\n"
      "v_mfma_f32_32x32x16_bf16 v[64:79], v[172:175], v[252:255], v[64:79]\n"
      "v_rcp_f32_e64 v33, v33\n"
      "v_rcp_f32_e64 v34, v34\n"
      "v_rcp_f32_e64 v35, v35\n"
      "v_mfma_f32_32x32x16_bf16 v[80:95], v[164:167], v[248:251], 0\n"
      "v_rcp_f32_e64 v36, v36\n"
      "v_rcp_f32_e64 v37, v37\n"
      "v_rcp_f32_e64 v38, v38\n"
      "v_mfma_f32_32x32x16_bf16 v[80:95], v[176:179], v[252:255], v[80:95]\n"
      "v_rcp_f32_e64 v39, v39\n"
      "v_rcp_f32_e64 v40, v40\n"
      "v_rcp_f32_e64 v41, v41\n"
      "v_mfma_f32_32x32x16_bf16 v[96:111], v[168:171], v[248:251], 0\n"
      "v_rcp_f32_e64 v42, v42\n"
      "v_rcp_f32_e64 v43, v43\n"
      "v_rcp_f32_e64 v44, v44\n"
      "v_mfma_f32_32x32x16_bf16 v[96:111], v[180:183], v[252:255], v[96:111]\n"
      "s_waitcnt vmcnt(0)\n"
      "s_setprio 3\n"
      "s_barrier\n"
      "s_nop 0\n"
      "v_rcp_f32_e64 v45, v45\n"
      "v_rcp_f32_e64 v46, v46\n"
      "v_rcp_f32_e32 v47, v47\n"
      "s_setprio 0\n"
      "ds_read_b64_tr_b16 v[136:137], v0 offset:36864\n"
      "ds_read_b64_tr_b16 v[138:139], v0 offset:37888\n"
      "ds_read_b64_tr_b16 v[140:141], v0 offset:38912\n"
      "ds_read_b64_tr_b16 v[142:143], v0 offset:39936\n"
      "ds_read_b64_tr_b16 v[144:145], v0 offset:40960\n"
      "ds_read_b64_tr_b16 v[146:147], v0 offset:41984\n"
      "ds_read_b64_tr_b16 v[148:149], v0 offset:43008\n"
      "ds_read_b64_tr_b16 v[150:151], v0 offset:44032\n"
      "ds_read_b64_tr_b16 v[152:153], v0 offset:45056\n"
      "ds_read_b64_tr_b16 v[154:155], v0 offset:46080\n"
      "ds_read_b64_tr_b16 v[156:157], v0 offset:47104\n"
      "ds_read_b64_tr_b16 v[158:159], v0 offset:48128\n"
      "ds_read_b64_tr_b16 v[160:161], v1 offset:30720\n"
      "ds_read_b64_tr_b16 v[162:163], v1 offset:32256\n"
      "ds_read_b64_tr_b16 v[164:165], v1 offset:30784\n"
      "ds_read_b64_tr_b16 v[166:167], v1 offset:32320\n"
      "ds_read_b64_tr_b16 v[168:169], v1 offset:30848\n"
      "ds_read_b64_tr_b16 v[170:171], v1 offset:32384\n"
      "ds_read_b64_tr_b16 v[172:173], v1 offset:33792\n"
      "ds_read_b64_tr_b16 v[174:175], v1 offset:35328\n"
      "ds_read_b64_tr_b16 v[176:177], v1 offset:33856\n"
      "ds_read_b64_tr_b16 v[178:179], v1 offset:35392\n"
      "ds_read_b64_tr_b16 v[180:181], v1 offset:33920\n"
      "ds_read_b64_tr_b16 v[182:183], v1 offset:35456\n"
      "s_lshl1_add_u32 m0, s79, 0\n"
      "s_add_u32 s97, s58, 0\n"
      "buffer_load_dwordx4 v28, s[36:39], s97 offen lds\n"
      "s_add_u32 m0, m0, 1024\n"
      "s_nop 0\n"
      "s_nop 0\n"
      "buffer_load_dwordx4 v29, s[36:39], s97 offen lds\n"
      "v_pk_mul_f32 v[32:33], v[32:33], v[200:201]\n"
      "v_pk_mul_f32 v[34:35], v[34:35], v[202:203]\n"
      "v_pk_mul_f32 v[36:37], v[36:37], v[204:205]\n"
      "v_pk_mul_f32 v[38:39], v[38:39], v[206:207]\n"
      "v_pk_mul_f32 v[40:41], v[40:41], v[208:209]\n"
      "v_pk_mul_f32 v[42:43], v[42:43], v[210:211]\n"
      "v_pk_mul_f32 v[44:45], v[44:45], v[212:213]\n"
      "v_pk_mul_f32 v[46:47], v[46:47], v[214:215]\n"
      "v_pk_fma_f32 v[48:49], v[48:49], s[54:55], s[54:55]\n"
      "v_pk_fma_f32 v[50:51], v[50:51], s[54:55], s[54:55]\n"
      "v_pk_fma_f32 v[52:53], v[52:53], s[54:55], s[54:55]\n"
      "v_pk_fma_f32 v[54:55], v[54:55], s[54:55], s[54:55]\n"
      "v_pk_fma_f32 v[56:57], v[56:57], s[54:55], s[54:55]\n"
      "v_pk_fma_f32 v[58:59], v[58:59], s[54:55], s[54:55]\n"
      "v_pk_fma_f32 v[60:61], v[60:61], s[54:55], s[54:55]\n"
      "v_pk_fma_f32 v[62:63], v[62:63], s[54:55], s[54:55]\n"
      "v_pk_fma_f32 v[232:233], v[184:185], v[184:185], s[52:53]\n"
      "v_pk_fma_f32 v[234:235], v[186:187], v[186:187], s[52:53]\n"
      "v_pk_fma_f32 v[236:237], v[188:189], v[188:189], s[52:53]\n"
      "v_pk_fma_f32 v[238:239], v[190:191], v[190:191], s[52:53]\n"
      "v_pk_fma_f32 v[240:241], v[192:193], v[192:193], s[52:53]\n"
      "v_pk_fma_f32 v[242:243], v[194:195], v[194:195], s[52:53]\n"
      "v_pk_fma_f32 v[244:245], v[196:197], v[196:197], s[52:53]\n"
      "v_pk_fma_f32 v[246:247], v[198:199], v[198:199], s[52:53]\n"
      "v_pk_mul_f32 v[232:233], v[232:233], v[184:185]\n"
      "v_pk_mul_f32 v[234:235], v[234:235], v[186:187]\n"
      "v_pk_mul_f32 v[236:237], v[236:237], v[188:189]\n"
      "v_pk_mul_f32 v[238:239], v[238:239], v[190:191]\n"
      "v_pk_mul_f32 v[240:241], v[240:241], v[192:193]\n"
      "v_pk_mul_f32 v[242:243], v[242:243], v[194:195]\n"
      "v_pk_mul_f32 v[244:245], v[244:245], v[196:197]\n"
      "v_pk_mul_f32 v[246:247], v[246:247], v[198:199]\n"
      "s_waitcnt lgkmcnt(12)\n"
      "s_nop 0\n"
      "v_mfma_f32_32x32x16_bf16 v[200:215], v[136:139], v[112:115], 0\n"
      "v_cvt_pk_bf16_f32 v248, v32, v33\n"
      "v_cvt_pk_bf16_f32 v249, v34, v35\n"
      "v_exp_f32_e64 v232, v232\n"
      "v_exp_f32_e64 v233, v233\n"
      "v_mfma_f32_32x32x16_bf16 v[200:215], v[140:143], v[116:119], v[200:215]\n"
      "v_cvt_pk_bf16_f32 v250, v36, v37\n"
      "v_cvt_pk_bf16_f32 v251, v38, v39\n"
      "v_exp_f32_e64 v234, v234\n"
      "v_exp_f32_e64 v235, v235\n"
      "v_mfma_f32_32x32x16_bf16 v[200:215], v[144:147], v[120:123], v[200:215]\n"
      "v_cvt_pk_bf16_f32 v252, v40, v41\n"
      "v_cvt_pk_bf16_f32 v253, v42, v43\n"
      "v_exp_f32_e64 v236, v236\n"
      "v_exp_f32_e64 v237, v237\n"
      "v_mfma_f32_32x32x16_bf16 v[200:215], v[148:151], v[124:127], v[200:215]\n"
      "v_cvt_pk_bf16_f32 v254, v44, v45\n"
      "v_cvt_pk_bf16_f32 v255, v46, v47\n"
      "v_exp_f32_e64 v238, v238\n"
      "v_exp_f32_e64 v239, v239\n"
      "v_mfma_f32_32x32x16_bf16 v[200:215], v[152:155], v[128:131], v[200:215]\n"
      "v_exp_f32_e64 v240, v240\n"
      "v_exp_f32_e64 v241, v241\n"
      "v_exp_f32_e64 v242, v242\n"
      "v_mfma_f32_32x32x16_bf16 v[200:215], v[156:159], v[132:135], v[200:215]\n"
      "v_exp_f32_e64 v243, v243\n"
      "v_exp_f32_e64 v244, v244\n"
      "v_exp_f32_e32 v245, v245\n"
      "s_waitcnt lgkmcnt(0)\n"
      "v_mfma_f32_32x32x16_bf16 v[64:79], v[160:163], v[248:251], v[64:79]\n"
      "v_exp_f32_e64 v246, v246\n"
      "v_exp_f32_e64 v247, v247\n"
      "v_rcp_f32_e64 v48, v48\n"
      "v_mfma_f32_32x32x16_bf16 v[64:79], v[172:175], v[252:255], v[64:79]\n"
      "v_rcp_f32_e64 v49, v49\n"
      "v_rcp_f32_e64 v50, v50\n"
      "v_rcp_f32_e64 v51, v51\n"
      "v_mfma_f32_32x32x16_bf16 v[80:95], v[164:167], v[248:251], v[80:95]\n"
      "v_rcp_f32_e64 v52, v52\n"
      "v_rcp_f32_e64 v53, v53\n"
      "v_rcp_f32_e64 v54, v54\n"
      "v_mfma_f32_32x32x16_bf16 v[80:95], v[176:179], v[252:255], v[80:95]\n"
      "v_rcp_f32_e64 v55, v55\n"
      "v_rcp_f32_e64 v56, v56\n"
      "v_rcp_f32_e64 v57, v57\n"
      "v_mfma_f32_32x32x16_bf16 v[96:111], v[168:171], v[248:251], v[96:111]\n"
      "v_rcp_f32_e64 v58, v58\n"
      "v_rcp_f32_e64 v59, v59\n"
      "v_rcp_f32_e64 v60, v60\n"
      "v_mfma_f32_32x32x16_bf16 v[96:111], v[180:183], v[252:255], v[96:111]\n"
      "s_setprio 3\n"
      "s_barrier\n"
      "v_rcp_f32_e64 v61, v61\n"
      "v_rcp_f32_e64 v62, v62\n"
      "v_rcp_f32_e64 v63, v63\n"
      "s_setprio 0\n"
      "s_waitcnt vmcnt(0)\n"
      "ds_read_b128 v[8:11], v30\n"
      "ds_read_b128 v[12:15], v31\n"
      "ds_read_b64_tr_b16 v[136:137], v0 offset:37376\n"
      "ds_read_b64_tr_b16 v[138:139], v0 offset:38400\n"
      "ds_read_b64_tr_b16 v[140:141], v0 offset:39424\n"
      "ds_read_b64_tr_b16 v[142:143], v0 offset:40448\n"
      "ds_read_b64_tr_b16 v[144:145], v0 offset:41472\n"
      "ds_read_b64_tr_b16 v[146:147], v0 offset:42496\n"
      "ds_read_b64_tr_b16 v[148:149], v0 offset:43520\n"
      "ds_read_b64_tr_b16 v[150:151], v0 offset:44544\n"
      "ds_read_b64_tr_b16 v[152:153], v0 offset:45568\n"
      "ds_read_b64_tr_b16 v[154:155], v0 offset:46592\n"
      "ds_read_b64_tr_b16 v[156:157], v0 offset:47616\n"
      "ds_read_b64_tr_b16 v[158:159], v0 offset:48640\n"
      "ds_read_b64_tr_b16 v[160:161], v1 offset:0\n"
      "ds_read_b64_tr_b16 v[162:163], v1 offset:1536\n"
      "ds_read_b64_tr_b16 v[164:165], v1 offset:64\n"
      "ds_read_b64_tr_b16 v[166:167], v1 offset:1600\n"
      "ds_read_b64_tr_b16 v[168:169], v1 offset:128\n"
      "ds_read_b64_tr_b16 v[170:171], v1 offset:1664\n"
      "ds_read_b64_tr_b16 v[172:173], v1 offset:3072\n"
      "ds_read_b64_tr_b16 v[174:175], v1 offset:4608\n"
      "ds_read_b64_tr_b16 v[176:177], v1 offset:3136\n"
      "ds_read_b64_tr_b16 v[178:179], v1 offset:4672\n"
      "ds_read_b64_tr_b16 v[180:181], v1 offset:3200\n"
      "ds_read_b64_tr_b16 v[182:183], v1 offset:4736\n"
      "s_waitcnt lgkmcnt(14)\n"
      "s_lshl1_add_u32 m0, s79, 0\n"
      "s_add_u32 s97, s58, 64\n"
      "buffer_load_dwordx4 v28, s[36:39], s97 offen lds\n"
      "s_add_u32 m0, m0, 1024\n"
      "s_nop 0\n"
      "buffer_load_dwordx4 v29, s[36:39], s97 offen lds\n"
      "v_pk_mul_f32 v[48:49], v[48:49], v[216:217]\n"
      "v_pk_mul_f32 v[50:51], v[50:51], v[218:219]\n"
      "v_pk_mul_f32 v[52:53], v[52:53], v[220:221]\n"
      "v_pk_mul_f32 v[54:55], v[54:55], v[222:223]\n"
      "v_pk_mul_f32 v[56:57], v[56:57], v[224:225]\n"
      "v_pk_mul_f32 v[58:59], v[58:59], v[226:227]\n"
      "v_pk_mul_f32 v[60:61], v[60:61], v[228:229]\n"
      "v_pk_mul_f32 v[62:63], v[62:63], v[230:231]\n"
      "v_pk_fma_f32 v[232:233], v[232:233], s[54:55], s[54:55]\n"
      "v_pk_fma_f32 v[234:235], v[234:235], s[54:55], s[54:55]\n"
      "v_pk_fma_f32 v[236:237], v[236:237], s[54:55], s[54:55]\n"
      "v_pk_fma_f32 v[238:239], v[238:239], s[54:55], s[54:55]\n"
      "v_pk_fma_f32 v[240:241], v[240:241], s[54:55], s[54:55]\n"
      "v_pk_fma_f32 v[242:243], v[242:243], s[54:55], s[54:55]\n"
      "v_pk_fma_f32 v[244:245], v[244:245], s[54:55], s[54:55]\n"
      "v_pk_fma_f32 v[246:247], v[246:247], s[54:55], s[54:55]\n"
      "v_pk_fma_f32 v[32:33], v[200:201], v[200:201], s[52:53]\n"
      "v_pk_fma_f32 v[34:35], v[202:203], v[202:203], s[52:53]\n"
      "v_pk_fma_f32 v[36:37], v[204:205], v[204:205], s[52:53]\n"
      "v_pk_fma_f32 v[38:39], v[206:207], v[206:207], s[52:53]\n"
      "v_pk_fma_f32 v[40:41], v[208:209], v[208:209], s[52:53]\n"
      "v_pk_fma_f32 v[42:43], v[210:211], v[210:211], s[52:53]\n"
      "v_pk_fma_f32 v[44:45], v[212:213], v[212:213], s[52:53]\n"
      "v_pk_fma_f32 v[46:47], v[214:215], v[214:215], s[52:53]\n"
      "v_pk_mul_f32 v[32:33], v[32:33], v[200:201]\n"
      "v_pk_mul_f32 v[34:35], v[34:35], v[202:203]\n"
      "v_pk_mul_f32 v[36:37], v[36:37], v[204:205]\n"
      "v_pk_mul_f32 v[38:39], v[38:39], v[206:207]\n"
      "v_pk_mul_f32 v[40:41], v[40:41], v[208:209]\n"
      "v_pk_mul_f32 v[42:43], v[42:43], v[210:211]\n"
      "v_pk_mul_f32 v[44:45], v[44:45], v[212:213]\n"
      "v_pk_mul_f32 v[46:47], v[46:47], v[214:215]\n"
      "s_waitcnt lgkmcnt(12)\n"
      "s_nop 0\n"
      "v_mfma_f32_32x32x16_bf16 v[216:231], v[136:139], v[112:115], 0\n"
      "v_cvt_pk_bf16_f32 v248, v48, v49\n"
      "v_cvt_pk_bf16_f32 v249, v50, v51\n"
      "v_exp_f32_e64 v32, v32\n"
      "v_exp_f32_e64 v33, v33\n"
      "v_mfma_f32_32x32x16_bf16 v[216:231], v[140:143], v[116:119], v[216:231]\n"
      "v_cvt_pk_bf16_f32 v250, v52, v53\n"
      "v_cvt_pk_bf16_f32 v251, v54, v55\n"
      "v_exp_f32_e64 v34, v34\n"
      "v_exp_f32_e64 v35, v35\n"
      "v_mfma_f32_32x32x16_bf16 v[216:231], v[144:147], v[120:123], v[216:231]\n"
      "v_cvt_pk_bf16_f32 v252, v56, v57\n"
      "v_cvt_pk_bf16_f32 v253, v58, v59\n"
      "v_exp_f32_e64 v36, v36\n"
      "v_exp_f32_e64 v37, v37\n"
      "v_mfma_f32_32x32x16_bf16 v[216:231], v[148:151], v[124:127], v[216:231]\n"
      "v_cvt_pk_bf16_f32 v254, v60, v61\n"
      "v_cvt_pk_bf16_f32 v255, v62, v63\n"
      "v_exp_f32_e64 v38, v38\n"
      "v_exp_f32_e64 v39, v39\n"
      "v_mfma_f32_32x32x16_bf16 v[216:231], v[152:155], v[128:131], v[216:231]\n"
      "v_exp_f32_e64 v40, v40\n"
      "v_exp_f32_e64 v41, v41\n"
      "v_exp_f32_e64 v42, v42\n"
      "v_mfma_f32_32x32x16_bf16 v[216:231], v[156:159], v[132:135], v[216:231]\n"
      "v_exp_f32_e64 v43, v43\n"
      "v_exp_f32_e64 v44, v44\n"
      "v_exp_f32_e32 v45, v45\n"
      "s_waitcnt lgkmcnt(0)\n"
      "v_mfma_f32_32x32x16_bf16 v[64:79], v[160:163], v[248:251], v[64:79]\n"
      "v_exp_f32_e64 v46, v46\n"
      "v_exp_f32_e64 v47, v47\n"
      "v_rcp_f32_e64 v232, v232\n"
      "v_mfma_f32_32x32x16_bf16 v[64:79], v[172:175], v[252:255], v[64:79]\n"
      "v_rcp_f32_e64 v233, v233\n"
      "v_rcp_f32_e64 v234, v234\n"
      "v_rcp_f32_e64 v235, v235\n"
      "v_mfma_f32_32x32x16_bf16 v[80:95], v[164:167], v[248:251], v[80:95]\n"
      "v_rcp_f32_e64 v236, v236\n"
      "v_rcp_f32_e64 v237, v237\n"
      "v_rcp_f32_e64 v238, v238\n"
      "v_mfma_f32_32x32x16_bf16 v[80:95], v[176:179], v[252:255], v[80:95]\n"
      "v_rcp_f32_e64 v239, v239\n"
      "v_rcp_f32_e64 v240, v240\n"
      "v_rcp_f32_e64 v241, v241\n"
      "v_mfma_f32_32x32x16_bf16 v[96:111], v[168:171], v[248:251], v[96:111]\n"
      "v_rcp_f32_e64 v242, v242\n"
      "v_rcp_f32_e64 v243, v243\n"
      "v_rcp_f32_e64 v244, v244\n"
      "v_mfma_f32_32x32x16_bf16 v[96:111], v[180:183], v[252:255], v[96:111]\n"
      "s_setprio 3\n"
      "s_barrier\n"
      "v_rcp_f32_e64 v245, v245\n"
      "v_rcp_f32_e64 v246, v246\n"
      "v_rcp_f32_e32 v247, v247\n"
      "s_setprio 0\n"
      "ds_read_b64_tr_b16 v[136:137], v0 offset:12288\n"
      "ds_read_b64_tr_b16 v[138:139], v0 offset:13312\n"
      "ds_read_b64_tr_b16 v[140:141], v0 offset:14336\n"
      "ds_read_b64_tr_b16 v[142:143], v0 offset:15360\n"
      "ds_read_b64_tr_b16 v[144:145], v0 offset:16384\n"
      "ds_read_b64_tr_b16 v[146:147], v0 offset:17408\n"
      "ds_read_b64_tr_b16 v[148:149], v0 offset:18432\n"
      "ds_read_b64_tr_b16 v[150:151], v0 offset:19456\n"
      "ds_read_b64_tr_b16 v[152:153], v0 offset:20480\n"
      "ds_read_b64_tr_b16 v[154:155], v0 offset:21504\n"
      "ds_read_b64_tr_b16 v[156:157], v0 offset:22528\n"
      "ds_read_b64_tr_b16 v[158:159], v0 offset:23552\n"
      "ds_read_b64_tr_b16 v[160:161], v1 offset:6144\n"
      "ds_read_b64_tr_b16 v[162:163], v1 offset:7680\n"
      "ds_read_b64_tr_b16 v[164:165], v1 offset:6208\n"
      "ds_read_b64_tr_b16 v[166:167], v1 offset:7744\n"
      "ds_read_b64_tr_b16 v[168:169], v1 offset:6272\n"
      "ds_read_b64_tr_b16 v[170:171], v1 offset:7808\n"
      "ds_read_b64_tr_b16 v[172:173], v1 offset:9216\n"
      "ds_read_b64_tr_b16 v[174:175], v1 offset:10752\n"
      "ds_read_b64_tr_b16 v[176:177], v1 offset:9280\n"
      "ds_read_b64_tr_b16 v[178:179], v1 offset:10816\n"
      "ds_read_b64_tr_b16 v[180:181], v1 offset:9344\n"
      "ds_read_b64_tr_b16 v[182:183], v1 offset:10880\n"
      "v_pk_mul_f32 v[232:233], v[232:233], v[184:185]\n"
      "v_pk_mul_f32 v[234:235], v[234:235], v[186:187]\n"
      "v_pk_mul_f32 v[236:237], v[236:237], v[188:189]\n"
      "v_pk_mul_f32 v[238:239], v[238:239], v[190:191]\n"
      "v_pk_mul_f32 v[240:241], v[240:241], v[192:193]\n"
      "v_pk_mul_f32 v[242:243], v[242:243], v[194:195]\n"
      "v_pk_mul_f32 v[244:245], v[244:245], v[196:197]\n"
      "v_pk_mul_f32 v[246:247], v[246:247], v[198:199]\n"
      "v_pk_fma_f32 v[32:33], v[32:33], s[54:55], s[54:55]\n"
      "v_pk_fma_f32 v[34:35], v[34:35], s[54:55], s[54:55]\n"
      "v_pk_fma_f32 v[36:37], v[36:37], s[54:55], s[54:55]\n"
      "v_pk_fma_f32 v[38:39], v[38:39], s[54:55], s[54:55]\n"
      "v_pk_fma_f32 v[40:41], v[40:41], s[54:55], s[54:55]\n"
      "v_pk_fma_f32 v[42:43], v[42:43], s[54:55], s[54:55]\n"
      "v_pk_fma_f32 v[44:45], v[44:45], s[54:55], s[54:55]\n"
      "v_pk_fma_f32 v[46:47], v[46:47], s[54:55], s[54:55]\n"
      "v_pk_fma_f32 v[48:49], v[216:217], v[216:217], s[52:53]\n"
      "v_pk_fma_f32 v[50:51], v[218:219], v[218:219], s[52:53]\n"
      "v_pk_fma_f32 v[52:53], v[220:221], v[220:221], s[52:53]\n"
      "v_pk_fma_f32 v[54:55], v[222:223], v[222:223], s[52:53]\n"
      "v_pk_fma_f32 v[56:57], v[224:225], v[224:225], s[52:53]\n"
      "v_pk_fma_f32 v[58:59], v[226:227], v[226:227], s[52:53]\n"
      "v_pk_fma_f32 v[60:61], v[228:229], v[228:229], s[52:53]\n"
      "v_pk_fma_f32 v[62:63], v[230:231], v[230:231], s[52:53]\n"
      "v_pk_mul_f32 v[48:49], v[48:49], v[216:217]\n"
      "v_pk_mul_f32 v[50:51], v[50:51], v[218:219]\n"
      "v_pk_mul_f32 v[52:53], v[52:53], v[220:221]\n"
      "v_pk_mul_f32 v[54:55], v[54:55], v[222:223]\n"
      "v_pk_mul_f32 v[56:57], v[56:57], v[224:225]\n"
      "v_pk_mul_f32 v[58:59], v[58:59], v[226:227]\n"
      "v_pk_mul_f32 v[60:61], v[60:61], v[228:229]\n"
      "v_pk_mul_f32 v[62:63], v[62:63], v[230:231]\n"
      "v_add_u32 v1, 0xffff4000, v1\n"
      "s_waitcnt lgkmcnt(12)\n"
      "s_nop 0\n"
      "v_mfma_f32_32x32x16_bf16 v[184:199], v[136:139], v[112:115], 0\n"
      "v_cvt_pk_bf16_f32 v248, v232, v233\n"
      "v_cvt_pk_bf16_f32 v249, v234, v235\n"
      "v_exp_f32_e64 v48, v48\n"
      "v_exp_f32_e64 v49, v49\n"
      "v_mfma_f32_32x32x16_bf16 v[184:199], v[140:143], v[116:119], v[184:199]\n"
      "v_cvt_pk_bf16_f32 v250, v236, v237\n"
      "v_cvt_pk_bf16_f32 v251, v238, v239\n"
      "v_exp_f32_e64 v50, v50\n"
      "v_exp_f32_e64 v51, v51\n"
      "v_mfma_f32_32x32x16_bf16 v[184:199], v[144:147], v[120:123], v[184:199]\n"
      "v_cvt_pk_bf16_f32 v252, v240, v241\n"
      "v_cvt_pk_bf16_f32 v253, v242, v243\n"
      "v_exp_f32_e64 v52, v52\n"
      "v_exp_f32_e64 v53, v53\n"
      "v_mfma_f32_32x32x16_bf16 v[184:199], v[148:151], v[124:127], v[184:199]\n"
      "v_cvt_pk_bf16_f32 v254, v244, v245\n"
      "v_cvt_pk_bf16_f32 v255, v246, v247\n"
      "v_exp_f32_e64 v54, v54\n"
      "v_exp_f32_e64 v55, v55\n"
      "v_mfma_f32_32x32x16_bf16 v[184:199], v[152:155], v[128:131], v[184:199]\n"
      "v_exp_f32_e64 v56, v56\n"
      "v_exp_f32_e64 v57, v57\n"
      "v_exp_f32_e64 v58, v58\n"
      "v_mfma_f32_32x32x16_bf16 v[184:199], v[156:159], v[132:135], v[184:199]\n"
      "v_exp_f32_e64 v59, v59\n"
      "v_exp_f32_e64 v60, v60\n"
      "v_exp_f32_e32 v61, v61\n"
      "s_waitcnt lgkmcnt(0)\n"
      "v_mfma_f32_32x32x16_bf16 v[64:79], v[160:163], v[248:251], v[64:79]\n"
      "v_exp_f32_e64 v62, v62\n"
      "v_exp_f32_e64 v63, v63\n"
      "v_rcp_f32_e64 v32, v32\n"
      "v_mfma_f32_32x32x16_bf16 v[64:79], v[172:175], v[252:255], v[64:79]\n"
      "v_rcp_f32_e64 v33, v33\n"
      "v_rcp_f32_e64 v34, v34\n"
      "v_rcp_f32_e64 v35, v35\n"
      "v_mfma_f32_32x32x16_bf16 v[80:95], v[164:167], v[248:251], v[80:95]\n"
      "v_rcp_f32_e64 v36, v36\n"
      "v_rcp_f32_e64 v37, v37\n"
      "v_rcp_f32_e64 v38, v38\n"
      "v_mfma_f32_32x32x16_bf16 v[80:95], v[176:179], v[252:255], v[80:95]\n"
      "v_rcp_f32_e64 v39, v39\n"
      "v_rcp_f32_e64 v40, v40\n"
      "v_rcp_f32_e64 v41, v41\n"
      "v_mfma_f32_32x32x16_bf16 v[96:111], v[168:171], v[248:251], v[96:111]\n"
      "v_rcp_f32_e64 v42, v42\n"
      "v_rcp_f32_e64 v43, v43\n"
      "v_rcp_f32_e64 v44, v44\n"
      "v_mfma_f32_32x32x16_bf16 v[96:111], v[180:183], v[252:255], v[96:111]\n"
      "s_setprio 3\n"
      "s_barrier\n"
      "v_rcp_f32_e64 v45, v45\n"
      "v_rcp_f32_e64 v46, v46\n"
      "v_rcp_f32_e32 v47, v47\n"
      "s_setprio 0\n"
      "ds_read_b64_tr_b16 v[136:137], v0 offset:12800\n"
      "ds_read_b64_tr_b16 v[138:139], v0 offset:13824\n"
      "ds_read_b64_tr_b16 v[140:141], v0 offset:14848\n"
      "ds_read_b64_tr_b16 v[142:143], v0 offset:15872\n"
      "ds_read_b64_tr_b16 v[144:145], v0 offset:16896\n"
      "ds_read_b64_tr_b16 v[146:147], v0 offset:17920\n"
      "ds_read_b64_tr_b16 v[148:149], v0 offset:18944\n"
      "ds_read_b64_tr_b16 v[150:151], v0 offset:19968\n"
      "ds_read_b64_tr_b16 v[152:153], v0 offset:20992\n"
      "ds_read_b64_tr_b16 v[154:155], v0 offset:22016\n"
      "ds_read_b64_tr_b16 v[156:157], v0 offset:23040\n"
      "ds_read_b64_tr_b16 v[158:159], v0 offset:24064\n"
      "ds_read_b64_tr_b16 v[160:161], v1 offset:24576\n"
      "ds_read_b64_tr_b16 v[162:163], v1 offset:26112\n"
      "ds_read_b64_tr_b16 v[164:165], v1 offset:24640\n"
      "ds_read_b64_tr_b16 v[166:167], v1 offset:26176\n"
      "ds_read_b64_tr_b16 v[168:169], v1 offset:24704\n"
      "ds_read_b64_tr_b16 v[170:171], v1 offset:26240\n"
      "ds_read_b64_tr_b16 v[172:173], v1 offset:27648\n"
      "ds_read_b64_tr_b16 v[174:175], v1 offset:29184\n"
      "ds_read_b64_tr_b16 v[176:177], v1 offset:27712\n"
      "ds_read_b64_tr_b16 v[178:179], v1 offset:29248\n"
      "ds_read_b64_tr_b16 v[180:181], v1 offset:27776\n"
      "ds_read_b64_tr_b16 v[182:183], v1 offset:29312\n"
      "v_pk_mul_f32 v[32:33], v[32:33], v[200:201]\n"
      "v_pk_mul_f32 v[34:35], v[34:35], v[202:203]\n"
      "v_pk_mul_f32 v[36:37], v[36:37], v[204:205]\n"
      "v_pk_mul_f32 v[38:39], v[38:39], v[206:207]\n"
      "v_pk_mul_f32 v[40:41], v[40:41], v[208:209]\n"
      "v_pk_mul_f32 v[42:43], v[42:43], v[210:211]\n"
      "v_pk_mul_f32 v[44:45], v[44:45], v[212:213]\n"
      "v_pk_mul_f32 v[46:47], v[46:47], v[214:215]\n"
      "v_pk_fma_f32 v[48:49], v[48:49], s[54:55], s[54:55]\n"
      "v_pk_fma_f32 v[50:51], v[50:51], s[54:55], s[54:55]\n"
      "v_pk_fma_f32 v[52:53], v[52:53], s[54:55], s[54:55]\n"
      "v_pk_fma_f32 v[54:55], v[54:55], s[54:55], s[54:55]\n"
      "v_pk_fma_f32 v[56:57], v[56:57], s[54:55], s[54:55]\n"
      "v_pk_fma_f32 v[58:59], v[58:59], s[54:55], s[54:55]\n"
      "v_pk_fma_f32 v[60:61], v[60:61], s[54:55], s[54:55]\n"
      "v_pk_fma_f32 v[62:63], v[62:63], s[54:55], s[54:55]\n"
      "v_pk_fma_f32 v[232:233], v[184:185], v[184:185], s[52:53]\n"
      "v_pk_fma_f32 v[234:235], v[186:187], v[186:187], s[52:53]\n"
      "v_pk_fma_f32 v[236:237], v[188:189], v[188:189], s[52:53]\n"
      "v_pk_fma_f32 v[238:239], v[190:191], v[190:191], s[52:53]\n"
      "v_pk_fma_f32 v[240:241], v[192:193], v[192:193], s[52:53]\n"
      "v_pk_fma_f32 v[242:243], v[194:195], v[194:195], s[52:53]\n"
      "v_pk_fma_f32 v[244:245], v[196:197], v[196:197], s[52:53]\n"
      "v_pk_fma_f32 v[246:247], v[198:199], v[198:199], s[52:53]\n"
      "v_pk_mul_f32 v[232:233], v[232:233], v[184:185]\n"
      "v_pk_mul_f32 v[234:235], v[234:235], v[186:187]\n"
      "v_pk_mul_f32 v[236:237], v[236:237], v[188:189]\n"
      "v_pk_mul_f32 v[238:239], v[238:239], v[190:191]\n"
      "v_pk_mul_f32 v[240:241], v[240:241], v[192:193]\n"
      "v_pk_mul_f32 v[242:243], v[242:243], v[194:195]\n"
      "v_pk_mul_f32 v[244:245], v[244:245], v[196:197]\n"
      "v_pk_mul_f32 v[246:247], v[246:247], v[198:199]\n"
      "v_add_u32 v0, 0xffff4000, v0\n"
      "s_waitcnt lgkmcnt(12)\n"
      "s_nop 0\n"
      "v_mfma_f32_32x32x16_bf16 v[200:215], v[136:139], v[112:115], 0\n"
      "v_cvt_pk_bf16_f32 v248, v32, v33\n"
      "v_cvt_pk_bf16_f32 v249, v34, v35\n"
      "v_exp_f32_e64 v232, v232\n"
      "v_exp_f32_e64 v233, v233\n"
      "v_mfma_f32_32x32x16_bf16 v[200:215], v[140:143], v[116:119], v[200:215]\n"
      "v_cvt_pk_bf16_f32 v250, v36, v37\n"
      "v_cvt_pk_bf16_f32 v251, v38, v39\n"
      "v_exp_f32_e64 v234, v234\n"
      "v_exp_f32_e64 v235, v235\n"
      "v_mfma_f32_32x32x16_bf16 v[200:215], v[144:147], v[120:123], v[200:215]\n"
      "v_cvt_pk_bf16_f32 v252, v40, v41\n"
      "v_cvt_pk_bf16_f32 v253, v42, v43\n"
      "v_exp_f32_e64 v236, v236\n"
      "v_exp_f32_e64 v237, v237\n"
      "v_mfma_f32_32x32x16_bf16 v[200:215], v[148:151], v[124:127], v[200:215]\n"
      "v_cvt_pk_bf16_f32 v254, v44, v45\n"
      "v_cvt_pk_bf16_f32 v255, v46, v47\n"
      "v_exp_f32_e64 v238, v238\n"
      "v_exp_f32_e64 v239, v239\n"
      "v_mfma_f32_32x32x16_bf16 v[200:215], v[152:155], v[128:131], v[200:215]\n"
      "v_exp_f32_e64 v240, v240\n"
      "v_exp_f32_e64 v241, v241\n"
      "v_exp_f32_e64 v242, v242\n"
      "v_mfma_f32_32x32x16_bf16 v[200:215], v[156:159], v[132:135], v[200:215]\n"
      "v_exp_f32_e64 v243, v243\n"
      "v_exp_f32_e64 v244, v244\n"
      "v_exp_f32_e32 v245, v245\n"
      "s_waitcnt lgkmcnt(0)\n"
      "v_mfma_f32_32x32x16_bf16 v[64:79], v[160:163], v[248:251], v[64:79]\n"
      "v_exp_f32_e64 v246, v246\n"
      "v_exp_f32_e64 v247, v247\n"
      "v_rcp_f32_e64 v48, v48\n"
      "v_mfma_f32_32x32x16_bf16 v[64:79], v[172:175], v[252:255], v[64:79]\n"
      "v_rcp_f32_e64 v49, v49\n"
      "v_rcp_f32_e64 v50, v50\n"
      "v_rcp_f32_e64 v51, v51\n"
      "v_mfma_f32_32x32x16_bf16 v[80:95], v[164:167], v[248:251], v[80:95]\n"
      "v_rcp_f32_e64 v52, v52\n"
      "v_rcp_f32_e64 v53, v53\n"
      "v_rcp_f32_e64 v54, v54\n"
      "v_mfma_f32_32x32x16_bf16 v[80:95], v[176:179], v[252:255], v[80:95]\n"
      "v_rcp_f32_e64 v55, v55\n"
      "v_rcp_f32_e64 v56, v56\n"
      "v_rcp_f32_e64 v57, v57\n"
      "v_mfma_f32_32x32x16_bf16 v[96:111], v[168:171], v[248:251], v[96:111]\n"
      "v_rcp_f32_e64 v58, v58\n"
      "v_rcp_f32_e64 v59, v59\n"
      "v_rcp_f32_e64 v60, v60\n"
      "v_mfma_f32_32x32x16_bf16 v[96:111], v[180:183], v[252:255], v[96:111]\n"
      "s_setprio 3\n"
      "s_barrier\n"
      "v_rcp_f32_e64 v61, v61\n"
      "v_rcp_f32_e64 v62, v62\n"
      "v_rcp_f32_e64 v63, v63\n"
      "s_setprio 0\n"
      "s_waitcnt vmcnt(0)\n"
      "ds_read_b128 v[16:19], v30\n"
      "ds_read_b128 v[20:23], v31\n"
      "ds_read_b64_tr_b16 v[136:137], v0 offset:36864\n"
      "ds_read_b64_tr_b16 v[138:139], v0 offset:37888\n"
      "ds_read_b64_tr_b16 v[140:141], v0 offset:38912\n"
      "ds_read_b64_tr_b16 v[142:143], v0 offset:39936\n"
      "ds_read_b64_tr_b16 v[144:145], v0 offset:40960\n"
      "ds_read_b64_tr_b16 v[146:147], v0 offset:41984\n"
      "ds_read_b64_tr_b16 v[148:149], v0 offset:43008\n"
      "ds_read_b64_tr_b16 v[150:151], v0 offset:44032\n"
      "ds_read_b64_tr_b16 v[152:153], v0 offset:45056\n"
      "ds_read_b64_tr_b16 v[154:155], v0 offset:46080\n"
      "ds_read_b64_tr_b16 v[156:157], v0 offset:47104\n"
      "ds_read_b64_tr_b16 v[158:159], v0 offset:48128\n"
      "ds_read_b64_tr_b16 v[160:161], v1 offset:30720\n"
      "ds_read_b64_tr_b16 v[162:163], v1 offset:32256\n"
      "ds_read_b64_tr_b16 v[164:165], v1 offset:30784\n"
      "ds_read_b64_tr_b16 v[166:167], v1 offset:32320\n"
      "ds_read_b64_tr_b16 v[168:169], v1 offset:30848\n"
      "ds_read_b64_tr_b16 v[170:171], v1 offset:32384\n"
      "ds_read_b64_tr_b16 v[172:173], v1 offset:33792\n"
      "ds_read_b64_tr_b16 v[174:175], v1 offset:35328\n"
      "ds_read_b64_tr_b16 v[176:177], v1 offset:33856\n"
      "ds_read_b64_tr_b16 v[178:179], v1 offset:35392\n"
      "ds_read_b64_tr_b16 v[180:181], v1 offset:33920\n"
      "ds_read_b64_tr_b16 v[182:183], v1 offset:35456\n"
      "s_waitcnt lgkmcnt(14)\n"
      "s_lshl1_add_u32 m0, s79, 0\n"
      "s_add_u32 s97, s58, 128\n"
      "buffer_load_dwordx4 v28, s[36:39], s97 offen lds\n"
      "s_add_u32 m0, m0, 1024\n"
      "s_nop 0\n"
      "s_nop 0\n"
      "buffer_load_dwordx4 v29, s[36:39], s97 offen lds\n"
      "v_pk_mul_f32 v[48:49], v[48:49], v[216:217]\n"
      "v_pk_mul_f32 v[50:51], v[50:51], v[218:219]\n"
      "v_pk_mul_f32 v[52:53], v[52:53], v[220:221]\n"
      "v_pk_mul_f32 v[54:55], v[54:55], v[222:223]\n"
      "v_pk_mul_f32 v[56:57], v[56:57], v[224:225]\n"
      "v_pk_mul_f32 v[58:59], v[58:59], v[226:227]\n"
      "v_pk_mul_f32 v[60:61], v[60:61], v[228:229]\n"
      "v_pk_mul_f32 v[62:63], v[62:63], v[230:231]\n"
      "v_pk_fma_f32 v[232:233], v[232:233], s[54:55], s[54:55]\n"
      "v_pk_fma_f32 v[234:235], v[234:235], s[54:55], s[54:55]\n"
      "v_pk_fma_f32 v[236:237], v[236:237], s[54:55], s[54:55]\n"
      "v_pk_fma_f32 v[238:239], v[238:239], s[54:55], s[54:55]\n"
      "v_pk_fma_f32 v[240:241], v[240:241], s[54:55], s[54:55]\n"
      "v_pk_fma_f32 v[242:243], v[242:243], s[54:55], s[54:55]\n"
      "v_pk_fma_f32 v[244:245], v[244:245], s[54:55], s[54:55]\n"
      "v_pk_fma_f32 v[246:247], v[246:247], s[54:55], s[54:55]\n"
      "v_pk_fma_f32 v[32:33], v[200:201], v[200:201], s[52:53]\n"
      "v_pk_fma_f32 v[34:35], v[202:203], v[202:203], s[52:53]\n"
      "v_pk_fma_f32 v[36:37], v[204:205], v[204:205], s[52:53]\n"
      "v_pk_fma_f32 v[38:39], v[206:207], v[206:207], s[52:53]\n"
      "v_pk_fma_f32 v[40:41], v[208:209], v[208:209], s[52:53]\n"
      "v_pk_fma_f32 v[42:43], v[210:211], v[210:211], s[52:53]\n"
      "v_pk_fma_f32 v[44:45], v[212:213], v[212:213], s[52:53]\n"
      "v_pk_fma_f32 v[46:47], v[214:215], v[214:215], s[52:53]\n"
      "v_pk_mul_f32 v[32:33], v[32:33], v[200:201]\n"
      "v_pk_mul_f32 v[34:35], v[34:35], v[202:203]\n"
      "v_pk_mul_f32 v[36:37], v[36:37], v[204:205]\n"
      "v_pk_mul_f32 v[38:39], v[38:39], v[206:207]\n"
      "v_pk_mul_f32 v[40:41], v[40:41], v[208:209]\n"
      "v_pk_mul_f32 v[42:43], v[42:43], v[210:211]\n"
      "v_pk_mul_f32 v[44:45], v[44:45], v[212:213]\n"
      "v_pk_mul_f32 v[46:47], v[46:47], v[214:215]\n"
      "s_waitcnt lgkmcnt(12)\n"
      "s_nop 0\n"
      "v_mfma_f32_32x32x16_bf16 v[216:231], v[136:139], v[112:115], 0\n"
      "v_cvt_pk_bf16_f32 v248, v48, v49\n"
      "v_cvt_pk_bf16_f32 v249, v50, v51\n"
      "v_exp_f32_e64 v32, v32\n"
      "v_exp_f32_e64 v33, v33\n"
      "v_mfma_f32_32x32x16_bf16 v[216:231], v[140:143], v[116:119], v[216:231]\n"
      "v_cvt_pk_bf16_f32 v250, v52, v53\n"
      "v_cvt_pk_bf16_f32 v251, v54, v55\n"
      "v_exp_f32_e64 v34, v34\n"
      "v_exp_f32_e64 v35, v35\n"
      "v_mfma_f32_32x32x16_bf16 v[216:231], v[144:147], v[120:123], v[216:231]\n"
      "v_cvt_pk_bf16_f32 v252, v56, v57\n"
      "v_cvt_pk_bf16_f32 v253, v58, v59\n"
      "v_exp_f32_e64 v36, v36\n"
      "v_exp_f32_e64 v37, v37\n"
      "v_mfma_f32_32x32x16_bf16 v[216:231], v[148:151], v[124:127], v[216:231]\n"
      "v_cvt_pk_bf16_f32 v254, v60, v61\n"
      "v_cvt_pk_bf16_f32 v255, v62, v63\n"
      "v_exp_f32_e64 v38, v38\n"
      "v_exp_f32_e64 v39, v39\n"
      "v_mfma_f32_32x32x16_bf16 v[216:231], v[152:155], v[128:131], v[216:231]\n"
      "v_exp_f32_e64 v40, v40\n"
      "v_exp_f32_e64 v41, v41\n"
      "v_exp_f32_e64 v42, v42\n"
      "v_mfma_f32_32x32x16_bf16 v[216:231], v[156:159], v[132:135], v[216:231]\n"
      "v_exp_f32_e64 v43, v43\n"
      "v_exp_f32_e64 v44, v44\n"
      "v_exp_f32_e32 v45, v45\n"
      "s_waitcnt lgkmcnt(0)\n"
      "v_mfma_f32_32x32x16_bf16 v[64:79], v[160:163], v[248:251], v[64:79]\n"
      "v_exp_f32_e64 v46, v46\n"
      "v_exp_f32_e64 v47, v47\n"
      "v_rcp_f32_e64 v232, v232\n"
      "v_mfma_f32_32x32x16_bf16 v[64:79], v[172:175], v[252:255], v[64:79]\n"
      "v_rcp_f32_e64 v233, v233\n"
      "v_rcp_f32_e64 v234, v234\n"
      "v_rcp_f32_e64 v235, v235\n"
      "v_mfma_f32_32x32x16_bf16 v[80:95], v[164:167], v[248:251], v[80:95]\n"
      "v_rcp_f32_e64 v236, v236\n"
      "v_rcp_f32_e64 v237, v237\n"
      "v_rcp_f32_e64 v238, v238\n"
      "v_mfma_f32_32x32x16_bf16 v[80:95], v[176:179], v[252:255], v[80:95]\n"
      "v_rcp_f32_e64 v239, v239\n"
      "v_rcp_f32_e64 v240, v240\n"
      "v_rcp_f32_e64 v241, v241\n"
      "v_mfma_f32_32x32x16_bf16 v[96:111], v[168:171], v[248:251], v[96:111]\n"
      "v_rcp_f32_e64 v242, v242\n"
      "v_rcp_f32_e64 v243, v243\n"
      "v_rcp_f32_e64 v244, v244\n"
      "v_mfma_f32_32x32x16_bf16 v[96:111], v[180:183], v[252:255], v[96:111]\n"
      "s_setprio 3\n"
      "s_barrier\n"
      "v_rcp_f32_e64 v245, v245\n"
      "v_rcp_f32_e64 v246, v246\n"
      "v_rcp_f32_e32 v247, v247\n"
      "s_setprio 0\n"
      "ds_read_b64_tr_b16 v[136:137], v0 offset:37376\n"
      "ds_read_b64_tr_b16 v[138:139], v0 offset:38400\n"
      "ds_read_b64_tr_b16 v[140:141], v0 offset:39424\n"
      "ds_read_b64_tr_b16 v[142:143], v0 offset:40448\n"
      "ds_read_b64_tr_b16 v[144:145], v0 offset:41472\n"
      "ds_read_b64_tr_b16 v[146:147], v0 offset:42496\n"
      "ds_read_b64_tr_b16 v[148:149], v0 offset:43520\n"
      "ds_read_b64_tr_b16 v[150:151], v0 offset:44544\n"
      "ds_read_b64_tr_b16 v[152:153], v0 offset:45568\n"
      "ds_read_b64_tr_b16 v[154:155], v0 offset:46592\n"
      "ds_read_b64_tr_b16 v[156:157], v0 offset:47616\n"
      "ds_read_b64_tr_b16 v[158:159], v0 offset:48640\n"
      "ds_read_b64_tr_b16 v[160:161], v1 offset:0\n"
      "ds_read_b64_tr_b16 v[162:163], v1 offset:1536\n"
      "ds_read_b64_tr_b16 v[164:165], v1 offset:64\n"
      "ds_read_b64_tr_b16 v[166:167], v1 offset:1600\n"
      "ds_read_b64_tr_b16 v[168:169], v1 offset:128\n"
      "ds_read_b64_tr_b16 v[170:171], v1 offset:1664\n"
      "ds_read_b64_tr_b16 v[172:173], v1 offset:3072\n"
      "ds_read_b64_tr_b16 v[174:175], v1 offset:4608\n"
      "ds_read_b64_tr_b16 v[176:177], v1 offset:3136\n"
      "ds_read_b64_tr_b16 v[178:179], v1 offset:4672\n"
      "ds_read_b64_tr_b16 v[180:181], v1 offset:3200\n"
      "ds_read_b64_tr_b16 v[182:183], v1 offset:4736\n"
      "v_pk_mul_f32 v[232:233], v[232:233], v[184:185]\n"
      "v_pk_mul_f32 v[234:235], v[234:235], v[186:187]\n"
      "v_pk_mul_f32 v[236:237], v[236:237], v[188:189]\n"
      "v_pk_mul_f32 v[238:239], v[238:239], v[190:191]\n"
      "v_pk_mul_f32 v[240:241], v[240:241], v[192:193]\n"
      "v_pk_mul_f32 v[242:243], v[242:243], v[194:195]\n"
      "v_pk_mul_f32 v[244:245], v[244:245], v[196:197]\n"
      "v_pk_mul_f32 v[246:247], v[246:247], v[198:199]\n"
      "v_pk_fma_f32 v[32:33], v[32:33], s[54:55], s[54:55]\n"
      "v_pk_fma_f32 v[34:35], v[34:35], s[54:55], s[54:55]\n"
      "v_pk_fma_f32 v[36:37], v[36:37], s[54:55], s[54:55]\n"
      "v_pk_fma_f32 v[38:39], v[38:39], s[54:55], s[54:55]\n"
      "v_pk_fma_f32 v[40:41], v[40:41], s[54:55], s[54:55]\n"
      "v_pk_fma_f32 v[42:43], v[42:43], s[54:55], s[54:55]\n"
      "v_pk_fma_f32 v[44:45], v[44:45], s[54:55], s[54:55]\n"
      "v_pk_fma_f32 v[46:47], v[46:47], s[54:55], s[54:55]\n"
      "v_pk_fma_f32 v[48:49], v[216:217], v[216:217], s[52:53]\n"
      "v_pk_fma_f32 v[50:51], v[218:219], v[218:219], s[52:53]\n"
      "v_pk_fma_f32 v[52:53], v[220:221], v[220:221], s[52:53]\n"
      "v_pk_fma_f32 v[54:55], v[222:223], v[222:223], s[52:53]\n"
      "v_pk_fma_f32 v[56:57], v[224:225], v[224:225], s[52:53]\n"
      "v_pk_fma_f32 v[58:59], v[226:227], v[226:227], s[52:53]\n"
      "v_pk_fma_f32 v[60:61], v[228:229], v[228:229], s[52:53]\n"
      "v_pk_fma_f32 v[62:63], v[230:231], v[230:231], s[52:53]\n"
      "v_pk_mul_f32 v[48:49], v[48:49], v[216:217]\n"
      "v_pk_mul_f32 v[50:51], v[50:51], v[218:219]\n"
      "v_pk_mul_f32 v[52:53], v[52:53], v[220:221]\n"
      "v_pk_mul_f32 v[54:55], v[54:55], v[222:223]\n"
      "v_pk_mul_f32 v[56:57], v[56:57], v[224:225]\n"
      "v_pk_mul_f32 v[58:59], v[58:59], v[226:227]\n"
      "v_pk_mul_f32 v[60:61], v[60:61], v[228:229]\n"
      "v_pk_mul_f32 v[62:63], v[62:63], v[230:231]\n"
      "s_waitcnt lgkmcnt(12)\n"
      "s_nop 0\n"
      "v_mfma_f32_32x32x16_bf16 v[184:199], v[136:139], v[112:115], 0\n"
      "v_cvt_pk_bf16_f32 v248, v232, v233\n"
      "v_cvt_pk_bf16_f32 v249, v234, v235\n"
      "v_exp_f32_e64 v48, v48\n"
      "v_exp_f32_e64 v49, v49\n"
      "v_mfma_f32_32x32x16_bf16 v[184:199], v[140:143], v[116:119], v[184:199]\n"
      "v_cvt_pk_bf16_f32 v250, v236, v237\n"
      "v_cvt_pk_bf16_f32 v251, v238, v239\n"
      "v_exp_f32_e64 v50, v50\n"
      "v_exp_f32_e64 v51, v51\n"
      "v_mfma_f32_32x32x16_bf16 v[184:199], v[144:147], v[120:123], v[184:199]\n"
      "v_cvt_pk_bf16_f32 v252, v240, v241\n"
      "v_cvt_pk_bf16_f32 v253, v242, v243\n"
      "v_exp_f32_e64 v52, v52\n"
      "v_exp_f32_e64 v53, v53\n"
      "v_mfma_f32_32x32x16_bf16 v[184:199], v[148:151], v[124:127], v[184:199]\n"
      "v_cvt_pk_bf16_f32 v254, v244, v245\n"
      "v_cvt_pk_bf16_f32 v255, v246, v247\n"
      "v_exp_f32_e64 v54, v54\n"
      "v_exp_f32_e64 v55, v55\n"
      "v_mfma_f32_32x32x16_bf16 v[184:199], v[152:155], v[128:131], v[184:199]\n"
      "v_exp_f32_e64 v56, v56\n"
      "v_exp_f32_e64 v57, v57\n"
      "v_exp_f32_e64 v58, v58\n"
      "v_mfma_f32_32x32x16_bf16 v[184:199], v[156:159], v[132:135], v[184:199]\n"
      "v_exp_f32_e64 v59, v59\n"
      "v_exp_f32_e64 v60, v60\n"
      "v_exp_f32_e32 v61, v61\n"
      "s_waitcnt lgkmcnt(0)\n"
      "v_mfma_f32_32x32x16_bf16 v[64:79], v[160:163], v[248:251], v[64:79]\n"
      "v_exp_f32_e64 v62, v62\n"
      "v_exp_f32_e64 v63, v63\n"
      "v_rcp_f32_e64 v32, v32\n"
      "v_mfma_f32_32x32x16_bf16 v[64:79], v[172:175], v[252:255], v[64:79]\n"
      "v_rcp_f32_e64 v33, v33\n"
      "v_rcp_f32_e64 v34, v34\n"
      "v_rcp_f32_e64 v35, v35\n"
      "v_mfma_f32_32x32x16_bf16 v[80:95], v[164:167], v[248:251], v[80:95]\n"
      "v_rcp_f32_e64 v36, v36\n"
      "v_rcp_f32_e64 v37, v37\n"
      "v_rcp_f32_e64 v38, v38\n"
      "v_mfma_f32_32x32x16_bf16 v[80:95], v[176:179], v[252:255], v[80:95]\n"
      "v_rcp_f32_e64 v39, v39\n"
      "v_rcp_f32_e64 v40, v40\n"
      "v_rcp_f32_e64 v41, v41\n"
      "v_mfma_f32_32x32x16_bf16 v[96:111], v[168:171], v[248:251], v[96:111]\n"
      "v_rcp_f32_e64 v42, v42\n"
      "v_rcp_f32_e64 v43, v43\n"
      "v_rcp_f32_e64 v44, v44\n"
      "v_mfma_f32_32x32x16_bf16 v[96:111], v[180:183], v[252:255], v[96:111]\n"
      "s_setprio 3\n"
      "s_barrier\n"
      "v_rcp_f32_e64 v45, v45\n"
      "v_rcp_f32_e64 v46, v46\n"
      "v_rcp_f32_e32 v47, v47\n"
      "s_setprio 0\n"
      "ds_read_b64_tr_b16 v[136:137], v0 offset:12288\n"
      "ds_read_b64_tr_b16 v[138:139], v0 offset:13312\n"
      "ds_read_b64_tr_b16 v[140:141], v0 offset:14336\n"
      "ds_read_b64_tr_b16 v[142:143], v0 offset:15360\n"
      "ds_read_b64_tr_b16 v[144:145], v0 offset:16384\n"
      "ds_read_b64_tr_b16 v[146:147], v0 offset:17408\n"
      "ds_read_b64_tr_b16 v[148:149], v0 offset:18432\n"
      "ds_read_b64_tr_b16 v[150:151], v0 offset:19456\n"
      "ds_read_b64_tr_b16 v[152:153], v0 offset:20480\n"
      "ds_read_b64_tr_b16 v[154:155], v0 offset:21504\n"
      "ds_read_b64_tr_b16 v[156:157], v0 offset:22528\n"
      "ds_read_b64_tr_b16 v[158:159], v0 offset:23552\n"
      "ds_read_b64_tr_b16 v[160:161], v1 offset:6144\n"
      "ds_read_b64_tr_b16 v[162:163], v1 offset:7680\n"
      "ds_read_b64_tr_b16 v[164:165], v1 offset:6208\n"
      "ds_read_b64_tr_b16 v[166:167], v1 offset:7744\n"
      "ds_read_b64_tr_b16 v[168:169], v1 offset:6272\n"
      "ds_read_b64_tr_b16 v[170:171], v1 offset:7808\n"
      "ds_read_b64_tr_b16 v[172:173], v1 offset:9216\n"
      "ds_read_b64_tr_b16 v[174:175], v1 offset:10752\n"
      "ds_read_b64_tr_b16 v[176:177], v1 offset:9280\n"
      "ds_read_b64_tr_b16 v[178:179], v1 offset:10816\n"
      "ds_read_b64_tr_b16 v[180:181], v1 offset:9344\n"
      "ds_read_b64_tr_b16 v[182:183], v1 offset:10880\n"
      "v_pk_mul_f32 v[32:33], v[32:33], v[200:201]\n"
      "v_pk_mul_f32 v[34:35], v[34:35], v[202:203]\n"
      "v_pk_mul_f32 v[36:37], v[36:37], v[204:205]\n"
      "v_pk_mul_f32 v[38:39], v[38:39], v[206:207]\n"
      "v_pk_mul_f32 v[40:41], v[40:41], v[208:209]\n"
      "v_pk_mul_f32 v[42:43], v[42:43], v[210:211]\n"
      "v_pk_mul_f32 v[44:45], v[44:45], v[212:213]\n"
      "v_pk_mul_f32 v[46:47], v[46:47], v[214:215]\n"
      "v_pk_fma_f32 v[48:49], v[48:49], s[54:55], s[54:55]\n"
      "v_pk_fma_f32 v[50:51], v[50:51], s[54:55], s[54:55]\n"
      "v_pk_fma_f32 v[52:53], v[52:53], s[54:55], s[54:55]\n"
      "v_pk_fma_f32 v[54:55], v[54:55], s[54:55], s[54:55]\n"
      "v_pk_fma_f32 v[56:57], v[56:57], s[54:55], s[54:55]\n"
      "v_pk_fma_f32 v[58:59], v[58:59], s[54:55], s[54:55]\n"
      "v_pk_fma_f32 v[60:61], v[60:61], s[54:55], s[54:55]\n"
      "v_pk_fma_f32 v[62:63], v[62:63], s[54:55], s[54:55]\n"
      "v_pk_fma_f32 v[232:233], v[184:185], v[184:185], s[52:53]\n"
      "v_pk_fma_f32 v[234:235], v[186:187], v[186:187], s[52:53]\n"
      "v_pk_fma_f32 v[236:237], v[188:189], v[188:189], s[52:53]\n"
      "v_pk_fma_f32 v[238:239], v[190:191], v[190:191], s[52:53]\n"
      "v_pk_fma_f32 v[240:241], v[192:193], v[192:193], s[52:53]\n"
      "v_pk_fma_f32 v[242:243], v[194:195], v[194:195], s[52:53]\n"
      "v_pk_fma_f32 v[244:245], v[196:197], v[196:197], s[52:53]\n"
      "v_pk_fma_f32 v[246:247], v[198:199], v[198:199], s[52:53]\n"
      "v_pk_mul_f32 v[232:233], v[232:233], v[184:185]\n"
      "v_pk_mul_f32 v[234:235], v[234:235], v[186:187]\n"
      "v_pk_mul_f32 v[236:237], v[236:237], v[188:189]\n"
      "v_pk_mul_f32 v[238:239], v[238:239], v[190:191]\n"
      "v_pk_mul_f32 v[240:241], v[240:241], v[192:193]\n"
      "v_pk_mul_f32 v[242:243], v[242:243], v[194:195]\n"
      "v_pk_mul_f32 v[244:245], v[244:245], v[196:197]\n"
      "v_pk_mul_f32 v[246:247], v[246:247], v[198:199]\n"
      "v_add_u32 v1, 0xffff4000, v1\n"
      "s_waitcnt lgkmcnt(12)\n"
      "s_nop 0\n"
      "v_mfma_f32_32x32x16_bf16 v[200:215], v[136:139], v[112:115], 0\n"
      "v_cvt_pk_bf16_f32 v248, v32, v33\n"
      "v_cvt_pk_bf16_f32 v249, v34, v35\n"
      "v_exp_f32_e64 v232, v232\n"
      "v_exp_f32_e64 v233, v233\n"
      "v_mfma_f32_32x32x16_bf16 v[200:215], v[140:143], v[116:119], v[200:215]\n"
      "v_cvt_pk_bf16_f32 v250, v36, v37\n"
      "v_cvt_pk_bf16_f32 v251, v38, v39\n"
      "v_exp_f32_e64 v234, v234\n"
      "v_exp_f32_e64 v235, v235\n"
      "v_mfma_f32_32x32x16_bf16 v[200:215], v[144:147], v[120:123], v[200:215]\n"
      "v_cvt_pk_bf16_f32 v252, v40, v41\n"
      "v_cvt_pk_bf16_f32 v253, v42, v43\n"
      "v_exp_f32_e64 v236, v236\n"
      "v_exp_f32_e64 v237, v237\n"
      "v_mfma_f32_32x32x16_bf16 v[200:215], v[148:151], v[124:127], v[200:215]\n"
      "v_cvt_pk_bf16_f32 v254, v44, v45\n"
      "v_cvt_pk_bf16_f32 v255, v46, v47\n"
      "v_exp_f32_e64 v238, v238\n"
      "v_exp_f32_e64 v239, v239\n"
      "v_mfma_f32_32x32x16_bf16 v[200:215], v[152:155], v[128:131], v[200:215]\n"
      "v_exp_f32_e64 v240, v240\n"
      "v_exp_f32_e64 v241, v241\n"
      "v_exp_f32_e64 v242, v242\n"
      "v_mfma_f32_32x32x16_bf16 v[200:215], v[156:159], v[132:135], v[200:215]\n"
      "v_exp_f32_e64 v243, v243\n"
      "v_exp_f32_e64 v244, v244\n"
      "v_exp_f32_e32 v245, v245\n"
      "s_waitcnt lgkmcnt(0)\n"
      "v_mfma_f32_32x32x16_bf16 v[64:79], v[160:163], v[248:251], v[64:79]\n"
      "v_exp_f32_e64 v246, v246\n"
      "v_exp_f32_e64 v247, v247\n"
      "v_rcp_f32_e64 v48, v48\n"
      "v_mfma_f32_32x32x16_bf16 v[64:79], v[172:175], v[252:255], v[64:79]\n"
      "v_rcp_f32_e64 v49, v49\n"
      "v_rcp_f32_e64 v50, v50\n"
      "v_rcp_f32_e64 v51, v51\n"
      "v_mfma_f32_32x32x16_bf16 v[80:95], v[164:167], v[248:251], v[80:95]\n"
      "v_rcp_f32_e64 v52, v52\n"
      "v_rcp_f32_e64 v53, v53\n"
      "v_rcp_f32_e64 v54, v54\n"
      "v_mfma_f32_32x32x16_bf16 v[80:95], v[176:179], v[252:255], v[80:95]\n"
      "v_rcp_f32_e64 v55, v55\n"
      "v_rcp_f32_e64 v56, v56\n"
      "v_rcp_f32_e64 v57, v57\n"
      "v_mfma_f32_32x32x16_bf16 v[96:111], v[168:171], v[248:251], v[96:111]\n"
      "v_rcp_f32_e64 v58, v58\n"
      "v_rcp_f32_e64 v59, v59\n"
      "v_rcp_f32_e64 v60, v60\n"
      "v_mfma_f32_32x32x16_bf16 v[96:111], v[180:183], v[252:255], v[96:111]\n"
      "s_setprio 3\n"
      "s_barrier\n"
      "v_rcp_f32_e64 v61, v61\n"
      "v_rcp_f32_e64 v62, v62\n"
      "v_rcp_f32_e64 v63, v63\n"
      "s_setprio 0\n"
      "s_waitcnt vmcnt(0)\n"
      "ds_read_b128 v[24:27], v30\n"
      "ds_read_b128 v[28:31], v31\n"
      "ds_read_b64_tr_b16 v[136:137], v0 offset:12800\n"
      "ds_read_b64_tr_b16 v[138:139], v0 offset:13824\n"
      "ds_read_b64_tr_b16 v[140:141], v0 offset:14848\n"
      "ds_read_b64_tr_b16 v[142:143], v0 offset:15872\n"
      "ds_read_b64_tr_b16 v[144:145], v0 offset:16896\n"
      "ds_read_b64_tr_b16 v[146:147], v0 offset:17920\n"
      "ds_read_b64_tr_b16 v[148:149], v0 offset:18944\n"
      "ds_read_b64_tr_b16 v[150:151], v0 offset:19968\n"
      "ds_read_b64_tr_b16 v[152:153], v0 offset:20992\n"
      "ds_read_b64_tr_b16 v[154:155], v0 offset:22016\n"
      "ds_read_b64_tr_b16 v[156:157], v0 offset:23040\n"
      "ds_read_b64_tr_b16 v[158:159], v0 offset:24064\n"
      "ds_read_b64_tr_b16 v[160:161], v1 offset:24576\n"
      "ds_read_b64_tr_b16 v[162:163], v1 offset:26112\n"
      "ds_read_b64_tr_b16 v[164:165], v1 offset:24640\n"
      "ds_read_b64_tr_b16 v[166:167], v1 offset:26176\n"
      "ds_read_b64_tr_b16 v[168:169], v1 offset:24704\n"
      "ds_read_b64_tr_b16 v[170:171], v1 offset:26240\n"
      "ds_read_b64_tr_b16 v[172:173], v1 offset:27648\n"
      "ds_read_b64_tr_b16 v[174:175], v1 offset:29184\n"
      "ds_read_b64_tr_b16 v[176:177], v1 offset:27712\n"
      "ds_read_b64_tr_b16 v[178:179], v1 offset:29248\n"
      "ds_read_b64_tr_b16 v[180:181], v1 offset:27776\n"
      "ds_read_b64_tr_b16 v[182:183], v1 offset:29312\n"
      "v_pk_mul_f32 v[48:49], v[48:49], v[216:217]\n"
      "v_pk_mul_f32 v[50:51], v[50:51], v[218:219]\n"
      "v_pk_mul_f32 v[52:53], v[52:53], v[220:221]\n"
      "v_pk_mul_f32 v[54:55], v[54:55], v[222:223]\n"
      "v_pk_mul_f32 v[56:57], v[56:57], v[224:225]\n"
      "v_pk_mul_f32 v[58:59], v[58:59], v[226:227]\n"
      "v_pk_mul_f32 v[60:61], v[60:61], v[228:229]\n"
      "v_pk_mul_f32 v[62:63], v[62:63], v[230:231]\n"
      "v_pk_fma_f32 v[232:233], v[232:233], s[54:55], s[54:55]\n"
      "v_pk_fma_f32 v[234:235], v[234:235], s[54:55], s[54:55]\n"
      "v_pk_fma_f32 v[236:237], v[236:237], s[54:55], s[54:55]\n"
      "v_pk_fma_f32 v[238:239], v[238:239], s[54:55], s[54:55]\n"
      "v_pk_fma_f32 v[240:241], v[240:241], s[54:55], s[54:55]\n"
      "v_pk_fma_f32 v[242:243], v[242:243], s[54:55], s[54:55]\n"
      "v_pk_fma_f32 v[244:245], v[244:245], s[54:55], s[54:55]\n"
      "v_pk_fma_f32 v[246:247], v[246:247], s[54:55], s[54:55]\n"
      "v_pk_fma_f32 v[32:33], v[200:201], v[200:201], s[52:53]\n"
      "v_pk_fma_f32 v[34:35], v[202:203], v[202:203], s[52:53]\n"
      "v_pk_fma_f32 v[36:37], v[204:205], v[204:205], s[52:53]\n"
      "v_pk_fma_f32 v[38:39], v[206:207], v[206:207], s[52:53]\n"
      "v_pk_fma_f32 v[40:41], v[208:209], v[208:209], s[52:53]\n"
      "v_pk_fma_f32 v[42:43], v[210:211], v[210:211], s[52:53]\n"
      "v_pk_fma_f32 v[44:45], v[212:213], v[212:213], s[52:53]\n"
      "v_pk_fma_f32 v[46:47], v[214:215], v[214:215], s[52:53]\n"
      "v_pk_mul_f32 v[32:33], v[32:33], v[200:201]\n"
      "v_pk_mul_f32 v[34:35], v[34:35], v[202:203]\n"
      "v_pk_mul_f32 v[36:37], v[36:37], v[204:205]\n"
      "v_pk_mul_f32 v[38:39], v[38:39], v[206:207]\n"
      "v_pk_mul_f32 v[40:41], v[40:41], v[208:209]\n"
      "v_pk_mul_f32 v[42:43], v[42:43], v[210:211]\n"
      "v_pk_mul_f32 v[44:45], v[44:45], v[212:213]\n"
      "v_pk_mul_f32 v[46:47], v[46:47], v[214:215]\n"
      "s_waitcnt lgkmcnt(12)\n"
      "s_nop 0\n"
      "v_mfma_f32_32x32x16_bf16 v[216:231], v[136:139], v[112:115], 0\n"
      "v_cvt_pk_bf16_f32 v248, v48, v49\n"
      "v_cvt_pk_bf16_f32 v249, v50, v51\n"
      "v_exp_f32_e64 v32, v32\n"
      "v_exp_f32_e64 v33, v33\n"
      "v_mfma_f32_32x32x16_bf16 v[216:231], v[140:143], v[116:119], v[216:231]\n"
      "v_cvt_pk_bf16_f32 v250, v52, v53\n"
      "v_cvt_pk_bf16_f32 v251, v54, v55\n"
      "v_exp_f32_e64 v34, v34\n"
      "v_exp_f32_e64 v35, v35\n"
      "v_mfma_f32_32x32x16_bf16 v[216:231], v[144:147], v[120:123], v[216:231]\n"
      "v_cvt_pk_bf16_f32 v252, v56, v57\n"
      "v_cvt_pk_bf16_f32 v253, v58, v59\n"
      "v_exp_f32_e64 v36, v36\n"
      "v_exp_f32_e64 v37, v37\n"
      "v_mfma_f32_32x32x16_bf16 v[216:231], v[148:151], v[124:127], v[216:231]\n"
      "v_cvt_pk_bf16_f32 v254, v60, v61\n"
      "v_cvt_pk_bf16_f32 v255, v62, v63\n"
      "v_exp_f32_e64 v38, v38\n"
      "v_exp_f32_e64 v39, v39\n"
      "v_mfma_f32_32x32x16_bf16 v[216:231], v[152:155], v[128:131], v[216:231]\n"
      "v_exp_f32_e64 v40, v40\n"
      "v_exp_f32_e64 v41, v41\n"
      "v_exp_f32_e64 v42, v42\n"
      "v_mfma_f32_32x32x16_bf16 v[216:231], v[156:159], v[132:135], v[216:231]\n"
      "v_exp_f32_e64 v43, v43\n"
      "v_exp_f32_e64 v44, v44\n"
      "v_exp_f32_e32 v45, v45\n"
      "s_waitcnt lgkmcnt(0)\n"
      "v_mfma_f32_32x32x16_bf16 v[64:79], v[160:163], v[248:251], v[64:79]\n"
      "v_exp_f32_e64 v46, v46\n"
      "v_exp_f32_e64 v47, v47\n"
      "v_rcp_f32_e64 v232, v232\n"
      "v_mfma_f32_32x32x16_bf16 v[64:79], v[172:175], v[252:255], v[64:79]\n"
      "v_rcp_f32_e64 v233, v233\n"
      "v_rcp_f32_e64 v234, v234\n"
      "v_rcp_f32_e64 v235, v235\n"
      "v_mfma_f32_32x32x16_bf16 v[80:95], v[164:167], v[248:251], v[80:95]\n"
      "v_rcp_f32_e64 v236, v236\n"
      "v_rcp_f32_e64 v237, v237\n"
      "v_rcp_f32_e64 v238, v238\n"
      "v_mfma_f32_32x32x16_bf16 v[80:95], v[176:179], v[252:255], v[80:95]\n"
      "v_rcp_f32_e64 v239, v239\n"
      "v_rcp_f32_e64 v240, v240\n"
      "v_rcp_f32_e64 v241, v241\n"
      "v_mfma_f32_32x32x16_bf16 v[96:111], v[168:171], v[248:251], v[96:111]\n"
      "v_rcp_f32_e64 v242, v242\n"
      "v_rcp_f32_e64 v243, v243\n"
      "v_rcp_f32_e64 v244, v244\n"
      "v_mfma_f32_32x32x16_bf16 v[96:111], v[180:183], v[252:255], v[96:111]\n"
      "s_setprio 3\n"
      "s_barrier\n"
      "v_rcp_f32_e64 v245, v245\n"
      "v_rcp_f32_e64 v246, v246\n"
      "v_rcp_f32_e32 v247, v247\n"
      "s_setprio 0\n"
      "ds_read_b128 v[136:139], v6 offset:13312\n"
      "ds_read_b128 v[140:143], v6 offset:14336\n"
      "ds_read_b128 v[144:147], v6 offset:15360\n"
      "ds_read_b64_tr_b16 v[160:161], v1 offset:30720\n"
      "ds_read_b64_tr_b16 v[162:163], v1 offset:32256\n"
      "ds_read_b64_tr_b16 v[164:165], v1 offset:30784\n"
      "ds_read_b64_tr_b16 v[166:167], v1 offset:32320\n"
      "ds_read_b64_tr_b16 v[168:169], v1 offset:30848\n"
      "ds_read_b64_tr_b16 v[170:171], v1 offset:32384\n"
      "ds_read_b64_tr_b16 v[172:173], v1 offset:33792\n"
      "ds_read_b64_tr_b16 v[174:175], v1 offset:35328\n"
      "ds_read_b64_tr_b16 v[176:177], v1 offset:33856\n"
      "ds_read_b64_tr_b16 v[178:179], v1 offset:35392\n"
      "ds_read_b64_tr_b16 v[180:181], v1 offset:33920\n"
      "ds_read_b64_tr_b16 v[182:183], v1 offset:35456\n"
      "v_add_u32_e64 v5, s79, v6\n"
      "v_add_u32 v3, 13312, v5\n"
      "v_add_u32 v5, 7168, v5\n"
      "global_load_dwordx4 v[148:151], v5, %4\n"
      "global_load_dwordx2 v[152:153], v3, %4\n"
      "v_pk_mul_f32 v[232:233], v[232:233], v[184:185]\n"
      "v_pk_mul_f32 v[234:235], v[234:235], v[186:187]\n"
      "v_pk_mul_f32 v[236:237], v[236:237], v[188:189]\n"
      "v_pk_mul_f32 v[238:239], v[238:239], v[190:191]\n"
      "v_pk_mul_f32 v[240:241], v[240:241], v[192:193]\n"
      "v_pk_mul_f32 v[242:243], v[242:243], v[194:195]\n"
      "v_pk_mul_f32 v[244:245], v[244:245], v[196:197]\n"
      "v_pk_mul_f32 v[246:247], v[246:247], v[198:199]\n"
      "v_pk_fma_f32 v[32:33], v[32:33], s[54:55], s[54:55]\n"
      "v_pk_fma_f32 v[34:35], v[34:35], s[54:55], s[54:55]\n"
      "v_pk_fma_f32 v[36:37], v[36:37], s[54:55], s[54:55]\n"
      "v_pk_fma_f32 v[38:39], v[38:39], s[54:55], s[54:55]\n"
      "v_pk_fma_f32 v[40:41], v[40:41], s[54:55], s[54:55]\n"
      "v_pk_fma_f32 v[42:43], v[42:43], s[54:55], s[54:55]\n"
      "v_pk_fma_f32 v[44:45], v[44:45], s[54:55], s[54:55]\n"
      "v_pk_fma_f32 v[46:47], v[46:47], s[54:55], s[54:55]\n"
      "v_pk_fma_f32 v[48:49], v[216:217], v[216:217], s[52:53]\n"
      "v_pk_fma_f32 v[50:51], v[218:219], v[218:219], s[52:53]\n"
      "v_pk_fma_f32 v[52:53], v[220:221], v[220:221], s[52:53]\n"
      "v_pk_fma_f32 v[54:55], v[222:223], v[222:223], s[52:53]\n"
      "v_pk_fma_f32 v[56:57], v[224:225], v[224:225], s[52:53]\n"
      "v_pk_fma_f32 v[58:59], v[226:227], v[226:227], s[52:53]\n"
      "v_pk_fma_f32 v[60:61], v[228:229], v[228:229], s[52:53]\n"
      "v_pk_fma_f32 v[62:63], v[230:231], v[230:231], s[52:53]\n"
      "v_pk_mul_f32 v[48:49], v[48:49], v[216:217]\n"
      "v_pk_mul_f32 v[50:51], v[50:51], v[218:219]\n"
      "v_pk_mul_f32 v[52:53], v[52:53], v[220:221]\n"
      "v_pk_mul_f32 v[54:55], v[54:55], v[222:223]\n"
      "v_pk_mul_f32 v[56:57], v[56:57], v[224:225]\n"
      "v_pk_mul_f32 v[58:59], v[58:59], v[226:227]\n"
      "v_pk_mul_f32 v[60:61], v[60:61], v[228:229]\n"
      "v_pk_mul_f32 v[62:63], v[62:63], v[230:231]\n"
      "s_waitcnt lgkmcnt(12)\n"
      "s_nop 0\n"
      "v_mfma_f32_32x32x16_bf16 v[64:79], v[136:139], v[8:11], v[64:79]\n"
      "v_cvt_pk_bf16_f32 v248, v232, v233\n"
      "v_cvt_pk_bf16_f32 v249, v234, v235\n"
      "v_exp_f32_e64 v48, v48\n"
      "v_exp_f32_e64 v49, v49\n"
      "v_mfma_f32_32x32x16_bf16 v[80:95], v[136:139], v[16:19], v[80:95]\n"
      "v_cvt_pk_bf16_f32 v250, v236, v237\n"
      "v_cvt_pk_bf16_f32 v251, v238, v239\n"
      "v_exp_f32_e64 v50, v50\n"
      "v_exp_f32_e64 v51, v51\n"
      "v_mfma_f32_32x32x16_bf16 v[96:111], v[136:139], v[24:27], v[96:111]\n"
      "v_cvt_pk_bf16_f32 v252, v240, v241\n"
      "v_cvt_pk_bf16_f32 v253, v242, v243\n"
      "v_exp_f32_e64 v52, v52\n"
      "v_exp_f32_e64 v53, v53\n"
      "v_mfma_f32_32x32x16_bf16 v[64:79], v[140:143], v[12:15], v[64:79]\n"
      "v_cvt_pk_bf16_f32 v254, v244, v245\n"
      "v_cvt_pk_bf16_f32 v255, v246, v247\n"
      "v_exp_f32_e64 v54, v54\n"
      "v_exp_f32_e64 v55, v55\n"
      "v_mfma_f32_32x32x16_bf16 v[80:95], v[140:143], v[20:23], v[80:95]\n"
      "v_exp_f32_e64 v56, v56\n"
      "v_exp_f32_e64 v57, v57\n"
      "v_exp_f32_e64 v58, v58\n"
      "v_mfma_f32_32x32x16_bf16 v[96:111], v[140:143], v[28:31], v[96:111]\n"
      "v_exp_f32_e64 v59, v59\n"
      "v_exp_f32_e64 v60, v60\n"
      "v_exp_f32_e32 v61, v61\n"
      "s_waitcnt lgkmcnt(0)\n"
      "v_mfma_f32_32x32x16_bf16 v[64:79], v[160:163], v[248:251], v[64:79]\n"
      "v_exp_f32_e64 v62, v62\n"
      "v_exp_f32_e64 v63, v63\n"
      "v_rcp_f32_e64 v32, v32\n"
      "v_mfma_f32_32x32x16_bf16 v[64:79], v[172:175], v[252:255], v[64:79]\n"
      "v_rcp_f32_e64 v33, v33\n"
      "v_rcp_f32_e64 v34, v34\n"
      "v_rcp_f32_e64 v35, v35\n"
      "v_mfma_f32_32x32x16_bf16 v[80:95], v[164:167], v[248:251], v[80:95]\n"
      "v_rcp_f32_e64 v36, v36\n"
      "v_rcp_f32_e64 v37, v37\n"
      "v_rcp_f32_e64 v38, v38\n"
      "v_mfma_f32_32x32x16_bf16 v[80:95], v[176:179], v[252:255], v[80:95]\n"
      "v_rcp_f32_e64 v39, v39\n"
      "v_rcp_f32_e64 v40, v40\n"
      "v_rcp_f32_e64 v41, v41\n"
      "v_mfma_f32_32x32x16_bf16 v[96:111], v[168:171], v[248:251], v[96:111]\n"
      "v_rcp_f32_e64 v42, v42\n"
      "v_rcp_f32_e64 v43, v43\n"
      "v_rcp_f32_e64 v44, v44\n"
      "v_mfma_f32_32x32x16_bf16 v[96:111], v[180:183], v[252:255], v[96:111]\n"
      "s_setprio 3\n"
      "s_barrier\n"
      "v_rcp_f32_e64 v45, v45\n"
      "v_rcp_f32_e64 v46, v46\n"
      "v_rcp_f32_e32 v47, v47\n"
      "s_setprio 0\n"
      "ds_read_b64_tr_b16 v[160:161], v1 offset:0\n"
      "ds_read_b64_tr_b16 v[162:163], v1 offset:1536\n"
      "ds_read_b64_tr_b16 v[164:165], v1 offset:64\n"
      "ds_read_b64_tr_b16 v[166:167], v1 offset:1600\n"
      "ds_read_b64_tr_b16 v[168:169], v1 offset:128\n"
      "ds_read_b64_tr_b16 v[170:171], v1 offset:1664\n"
      "ds_read_b64_tr_b16 v[172:173], v1 offset:3072\n"
      "ds_read_b64_tr_b16 v[174:175], v1 offset:4608\n"
      "ds_read_b64_tr_b16 v[176:177], v1 offset:3136\n"
      "ds_read_b64_tr_b16 v[178:179], v1 offset:4672\n"
      "ds_read_b64_tr_b16 v[180:181], v1 offset:3200\n"
      "ds_read_b64_tr_b16 v[182:183], v1 offset:4736\n"
      "v_pk_mul_f32 v[32:33], v[32:33], v[200:201]\n"
      "v_pk_mul_f32 v[34:35], v[34:35], v[202:203]\n"
      "v_pk_mul_f32 v[36:37], v[36:37], v[204:205]\n"
      "v_pk_mul_f32 v[38:39], v[38:39], v[206:207]\n"
      "v_pk_mul_f32 v[40:41], v[40:41], v[208:209]\n"
      "v_pk_mul_f32 v[42:43], v[42:43], v[210:211]\n"
      "v_pk_mul_f32 v[44:45], v[44:45], v[212:213]\n"
      "v_pk_mul_f32 v[46:47], v[46:47], v[214:215]\n"
      "v_pk_fma_f32 v[48:49], v[48:49], s[54:55], s[54:55]\n"
      "v_pk_fma_f32 v[50:51], v[50:51], s[54:55], s[54:55]\n"
      "v_pk_fma_f32 v[52:53], v[52:53], s[54:55], s[54:55]\n"
      "v_pk_fma_f32 v[54:55], v[54:55], s[54:55], s[54:55]\n"
      "v_pk_fma_f32 v[56:57], v[56:57], s[54:55], s[54:55]\n"
      "v_pk_fma_f32 v[58:59], v[58:59], s[54:55], s[54:55]\n"
      "v_pk_fma_f32 v[60:61], v[60:61], s[54:55], s[54:55]\n"
      "v_pk_fma_f32 v[62:63], v[62:63], s[54:55], s[54:55]\n"
      "v_cvt_pk_bf16_f32 v248, v32, v33\n"
      "v_cvt_pk_bf16_f32 v249, v34, v35\n"
      "v_cvt_pk_bf16_f32 v250, v36, v37\n"
      "v_cvt_pk_bf16_f32 v251, v38, v39\n"
      "s_waitcnt lgkmcnt(0)\n"
      "s_nop 0\n"
      "v_mfma_f32_32x32x16_bf16 v[64:79], v[160:163], v[248:251], v[64:79]\n"
      "ds_read_b64_tr_b16 v[112:113], v1 offset:6144\n"
      "ds_read_b64_tr_b16 v[114:115], v1 offset:7680\n"
      "v_cvt_pk_bf16_f32 v252, v40, v41\n"
      "v_cvt_pk_bf16_f32 v253, v42, v43\n"
      "v_rcp_f32_e64 v48, v48\n"
      "v_rcp_f32_e64 v49, v49\n"
      "v_mfma_f32_32x32x16_bf16 v[80:95], v[164:167], v[248:251], v[80:95]\n"
      "ds_read_b64_tr_b16 v[116:117], v1 offset:6208\n"
      "ds_read_b64_tr_b16 v[118:119], v1 offset:7744\n"
      "v_cvt_pk_bf16_f32 v254, v44, v45\n"
      "v_cvt_pk_bf16_f32 v255, v46, v47\n"
      "v_rcp_f32_e64 v50, v50\n"
      "v_rcp_f32_e64 v51, v51\n"
      "v_mfma_f32_32x32x16_bf16 v[96:111], v[168:171], v[248:251], v[96:111]\n"
      "ds_read_b64_tr_b16 v[120:121], v1 offset:6272\n"
      "ds_read_b64_tr_b16 v[122:123], v1 offset:7808\n"
      "v_rcp_f32_e64 v52, v52\n"
      "v_rcp_f32_e64 v53, v53\n"
      "v_rcp_f32_e64 v54, v54\n"
      "v_mfma_f32_32x32x16_bf16 v[64:79], v[172:175], v[252:255], v[64:79]\n"
      "ds_read_b64_tr_b16 v[124:125], v1 offset:9216\n"
      "ds_read_b64_tr_b16 v[126:127], v1 offset:10752\n"
      "v_rcp_f32_e64 v55, v55\n"
      "v_rcp_f32_e64 v56, v56\n"
      "v_rcp_f32_e64 v57, v57\n"
      "v_mfma_f32_32x32x16_bf16 v[80:95], v[176:179], v[252:255], v[80:95]\n"
      "ds_read_b64_tr_b16 v[128:129], v1 offset:9280\n"
      "ds_read_b64_tr_b16 v[130:131], v1 offset:10816\n"
      "v_rcp_f32_e64 v58, v58\n"
      "v_rcp_f32_e64 v59, v59\n"
      "v_rcp_f32_e64 v60, v60\n"
      "v_mfma_f32_32x32x16_bf16 v[96:111], v[180:183], v[252:255], v[96:111]\n"
      "ds_read_b64_tr_b16 v[132:133], v1 offset:9344\n"
      "ds_read_b64_tr_b16 v[134:135], v1 offset:10880\n"
      "v_rcp_f32_e64 v61, v61\n"
      "v_rcp_f32_e64 v62, v62\n"
      "v_rcp_f32_e64 v63, v63\n"
      "v_pk_mul_f32 v[48:49], v[48:49], v[216:217]\n"
      "v_pk_mul_f32 v[50:51], v[50:51], v[218:219]\n"
      "v_pk_mul_f32 v[52:53], v[52:53], v[220:221]\n"
      "v_pk_mul_f32 v[54:55], v[54:55], v[222:223]\n"
      "v_pk_mul_f32 v[56:57], v[56:57], v[224:225]\n"
      "v_pk_mul_f32 v[58:59], v[58:59], v[226:227]\n"
      "v_pk_mul_f32 v[60:61], v[60:61], v[228:229]\n"
      "v_pk_mul_f32 v[62:63], v[62:63], v[230:231]\n"
      "v_add_u32 v0, s75, v144\n"
      "v_xor_b32 v1, 32, v0\n"
      "v_add_u32_e64 v2, s75, v6\n"
      "v_cvt_pk_bf16_f32 v248, v48, v49\n"
      "v_cvt_pk_bf16_f32 v249, v50, v51\n"
      "v_cvt_pk_bf16_f32 v250, v52, v53\n"
      "v_cvt_pk_bf16_f32 v251, v54, v55\n"
      "v_cvt_pk_bf16_f32 v252, v56, v57\n"
      "v_cvt_pk_bf16_f32 v253, v58, v59\n"
      "v_cvt_pk_bf16_f32 v254, v60, v61\n"
      "v_cvt_pk_bf16_f32 v255, v62, v63\n"
      "s_waitcnt lgkmcnt(0)\n"
      "s_nop 0\n"
      "v_mfma_f32_32x32x16_bf16 v[64:79], v[112:115], v[248:251], v[64:79]\n"
      "v_mfma_f32_32x32x16_bf16 v[64:79], v[124:127], v[252:255], v[64:79]\n"
      "v_mfma_f32_32x32x16_bf16 v[80:95], v[116:119], v[248:251], v[80:95]\n"
      "v_mfma_f32_32x32x16_bf16 v[80:95], v[128:131], v[252:255], v[80:95]\n"
      "s_nop 9\n"
      "s_nop 0\n"
      "v_cvt_pk_bf16_f32 v8, v64, v65\n"
      "v_cvt_pk_bf16_f32 v9, v66, v67\n"
      "v_cvt_pk_bf16_f32 v10, v68, v69\n"
      "v_cvt_pk_bf16_f32 v11, v70, v71\n"
      "ds_write_b128 v0, v[8:11] offset:0\n"
      "v_mfma_f32_32x32x16_bf16 v[96:111], v[120:123], v[248:251], v[96:111]\n"
      "v_cvt_pk_bf16_f32 v12, v72, v73\n"
      "v_cvt_pk_bf16_f32 v13, v74, v75\n"
      "v_cvt_pk_bf16_f32 v14, v76, v77\n"
      "v_cvt_pk_bf16_f32 v15, v78, v79\n"
      "ds_write_b128 v1, v[12:15] offset:0\n"
      "v_mfma_f32_32x32x16_bf16 v[96:111], v[132:135], v[252:255], v[96:111]\n"
      "v_cvt_pk_bf16_f32 v16, v80, v81\n"
      "v_cvt_pk_bf16_f32 v17, v82, v83\n"
      "v_cvt_pk_bf16_f32 v18, v84, v85\n"
      "v_cvt_pk_bf16_f32 v19, v86, v87\n"
      "ds_write_b128 v0, v[16:19] offset:64\n"
      "v_cvt_pk_bf16_f32 v20, v88, v89\n"
      "v_cvt_pk_bf16_f32 v21, v90, v91\n"
      "v_cvt_pk_bf16_f32 v22, v92, v93\n"
      "v_cvt_pk_bf16_f32 v23, v94, v95\n"
      "ds_write_b128 v1, v[20:23] offset:64\n"
      "s_nop 1\n"
      "s_nop 0\n"
      "v_cvt_pk_bf16_f32 v24, v96, v97\n"
      "v_cvt_pk_bf16_f32 v25, v98, v99\n"
      "v_cvt_pk_bf16_f32 v26, v100, v101\n"
      "v_cvt_pk_bf16_f32 v27, v102, v103\n"
      "ds_write_b128 v0, v[24:27] offset:128\n"
      "v_cvt_pk_bf16_f32 v28, v104, v105\n"
      "v_cvt_pk_bf16_f32 v29, v106, v107\n"
      "v_cvt_pk_bf16_f32 v30, v108, v109\n"
      "v_cvt_pk_bf16_f32 v31, v110, v111\n"
      "ds_write_b128 v1, v[28:31] offset:128\n"
      "ds_read_b128 v[184:187], v2 offset:0\n"
      "ds_read_b128 v[188:191], v2 offset:1024\n"
      "ds_read_b128 v[192:195], v2 offset:2048\n"
      "ds_read_b128 v[196:199], v2 offset:3072\n"
      "ds_read_b128 v[200:203], v2 offset:4096\n"
      "ds_read_b128 v[204:207], v2 offset:5120\n"
      "s_waitcnt vmcnt(0) lgkmcnt(5)\n"
      "buffer_store_dwordx4 v[184:187], v148, s[48:51], s58 offen nt\n"
      "s_waitcnt vmcnt(1) lgkmcnt(4)\n"
      "buffer_store_dwordx4 v[188:191], v149, s[48:51], s58 offen nt\n"
      "s_waitcnt vmcnt(2) lgkmcnt(3)\n"
      "buffer_store_dwordx4 v[192:195], v150, s[48:51], s58 offen nt\n"
      "s_waitcnt vmcnt(3) lgkmcnt(2)\n"
      "buffer_store_dwordx4 v[196:199], v151, s[48:51], s58 offen nt\n"
      "s_waitcnt vmcnt(4) lgkmcnt(1)\n"
      "buffer_store_dwordx4 v[200:203], v152, s[48:51], s58 offen nt\n"
      "s_waitcnt vmcnt(5) lgkmcnt(0)\n"
      "buffer_store_dwordx4 v[204:207], v153, s[48:51], s58 offen nt\n"
      ".Lcnx_done:\n"
      ".Lcnx_end:\n"
      :
      : "s"(__builtin_amdgcn_kernarg_segment_ptr()), "s"(blockIdx.x), "{v0}"(threadIdx.x), "s"((unsigned)(size_t)smem),
        "s"(cnx_tab)
      : "v1", "v2", "v3", "v4", "v5", "v6", "v7", "v8", "v9", "v10", "v11", "v12", "v13", "v14", "v15", "v16", "v17", "v18", "v19", "v20", "v21", "v22", "v23", "v24", "v25", "v26", "v27", "v28", "v29", "v30", "v31", "v32", "v33", "v34", "v35", "v36", "v37", "v38", "v39", "v40", "v41", "v42", "v43", "v44", "v45", "v46", "v47", "v48", "v49", "v50", "v51", "v52", "v53", "v54", "v55", "v56", "v57", "v58", "v59", "v60", "v61", "v62", "v63", "v64", "v65", "v66", "v67", "v68", "v69", "v70", "v71", "v72", "v73", "v74", "v75", "v76", "v77", "v78", "v79", "v80", "v81", "v82", "v83", "v84", "v85", "v86", "v87", "v88", "v89", "v90", "v91", "v92", "v93", "v94", "v95", "v96", "v97", "v98", "v99", "v100", "v101", "v102", "v103", "v104", "v105", "v106", "v107", "v108", "v109", "v110", "v111", "v112", "v113", "v114", "v115", "v116", "v117", "v118", "v119", "v120", "v121", "v122", "v123", "v124", "v125", "v126", "v127", "v128", "v129", "v130", "v131", "v132", "v133", "v134", "v135", "v136", "v137", "v138", "v139", "v140", "v141", "v142", "v143", "v144", "v145", "v146", "v147", "v148", "v149", "v150", "v151", "v152", "v153", "v154", "v155", "v156", "v157", "v158", "v159", "v160", "v161", "v162", "v163", "v164", "v165", "v166", "v167", "v168", "v169", "v170", "v171", "v172", "v173", "v174", "v175", "v176", "v177", "v178", "v179", "v180", "v181", "v182", "v183", "v184", "v185", "v186", "v187", "v188", "v189", "v190", "v191", "v192", "v193", "v194", "v195", "v196", "v197", "v198", "v199", "v200", "v201", "v202", "v203", "v204", "v205", "v206", "v207", "v208", "v209", "v210", "v211", "v212", "v213", "v214", "v215", "v216", "v217", "v218", "v219", "v220", "v221", "v222", "v223", "v224", "v225", "v226", "v227", "v228", "v229", "v230", "v231", "v232", "v233", "v234", "v235", "v236", "v237", "v238", "v239", "v240", "v241", "v242", "v243", "v244", "v245", "v246", "v247", "v248", "v249", "v250", "v251", "v252", "v253", "v254", "v255", "s36", "s37", "s38", "s39", "s40", "s41", "s42", "s43", "s44", "s45", "s46", "s47", "s48", "s49", "s50", "s51", "s52", "s53", "s54", "s55", "s56", "s57", "s58", "s59", "s60", "s61", "s62", "s63", "s64", "s65", "s66", "s67", "s68", "s69", "s70", "s71", "s72", "s73", "s74", "s75", "s76", "s77", "s78", "s79", "s80", "s81", "s82", "s83", "s84", "s85", "s86", "s87", "s88", "s89", "s90", "s91", "s92", "s93", "s94", "s95", "s96", "s97", "s98", "s99", "vcc", "scc", "memory");
}

extern "C" void kernel_launch(void* const* d_in, const int* in_sizes, int n_in, void* d_out, int out_size, void* d_ws,
                              size_t ws_size, hipStream_t stream) {
  convnext_block_kernel<<<256, 512, 0, stream>>>(d_in[0], d_in[1], d_in[2], d_in[3], d_out, d_ws);
}
```
